```python
import torch

def _softmax(x, axis=-1):
    return torch.softmax(x, dim=axis)

def _gelu(x):
    return torch.nn.functional.gelu(x, approximate='tanh')

def _silu(x):
    return torch.nn.functional.silu(x)

def _sigmoid(x):
    return torch.sigmoid(x)

def _layernorm(x, axis=-1, eps=1e-05):
    m = x.mean(dim=axis, keepdim=True)
    v = x.var(dim=axis, keepdim=True, unbiased=False)
    return (x - m) / torch.sqrt(v + eps)

def _scatter_add(tgt, idx, src, axis=0):
    return tgt.clone().index_add_(axis, idx, src)

def _segment_sum(src, seg, n):
    out = torch.zeros((n,) + src.shape[1:], dtype=src.dtype)
    return out.index_add_(0, seg, src)

def _pad(x, pw):
    flat = tuple((v for pair in reversed(pw) for v in pair))
    return torch.nn.functional.pad(x, flat)
kernel_name = 'adaLN_cond__memory_aug_recurrent_mem_tr__glu_gated_geglu__prompt'

def setup_inputs():

    _w = lambda a: (lambda t: t.to(torch.bfloat16) if t.is_floating_point() else t)(torch.as_tensor(a))

    def _A_setup():
        B, S, Dm = (1, 2048, 512)
        H, Dh = (32, 128)
        M, seg = (8, 32)
        seg = min(seg, S)
        S = S // seg * seg
        return {'x': _w((lambda *s: torch.randn(*s, dtype=torch.bfloat16) * 0.05)(B, S, Dm)), 'mem0': _w((lambda *s: torch.randn(*s, dtype=torch.bfloat16) * 0.8)(B, M, Dm)), 'Wq': _w((lambda *s: torch.randn(*s, dtype=torch.bfloat16) * 0.8)(Dm, H, Dh) * Dm ** (-0.5)), 'Wk': _w((lambda *s: torch.randn(*s, dtype=torch.bfloat16) * 0.8)(Dm, H, Dh) * Dm ** (-0.5)), 'Wv': _w((lambda *s: torch.randn(*s, dtype=torch.bfloat16) * 0.8)(Dm, H, Dh) * Dm ** (-0.5)), 'Wo': _w((lambda *s: torch.randn(*s, dtype=torch.bfloat16) * 0.8)(H, Dh, Dm) * (H * Dh) ** (-0.5)), 'seg_len': seg}
    _A_in = _A_setup()

    def _B_setup():
        B, S, Dm, Df = (1, 2048, 512, 1376)
        return {'x': _w((lambda *s: torch.randn(*s, dtype=torch.bfloat16) * 0.05)(B, S, Dm)), 'w_gate': _w((lambda *s: torch.randn(*s, dtype=torch.bfloat16) * 0.4)(Dm, Df) * Dm ** (-0.5)), 'w_up': _w((lambda *s: torch.randn(*s, dtype=torch.bfloat16) * 0.4)(Dm, Df) * Dm ** (-0.5)), 'w_down': _w((lambda *s: torch.randn(*s, dtype=torch.bfloat16) * 0.4)(Df, Dm) * Df ** (-0.5))}
    _B_in = _B_setup()
    return {'A_x': _A_in['x'], 'A_mem0': _A_in['mem0'], 'A_Wq': _A_in['Wq'], 'A_Wk': _A_in['Wk'], 'A_Wv': _A_in['Wv'], 'A_Wo': _A_in['Wo'], 'A_seg_len': _A_in['seg_len'], 'B_x': _B_in['x'], 'B_w_gate': _B_in['w_gate'], 'B_w_up': _B_in['w_up'], 'B_w_down': _B_in['w_down'], 'c': _w((lambda *s: torch.randn(*s, dtype=torch.bfloat16) * 0.2)(1, 512) * 8.0), 'w_mod': _w((lambda *s: torch.randn(*s, dtype=torch.bfloat16) * 0.2)(512, 6 * 512) * 512 ** (-0.5) * 8.0)}

def reference(A_x, A_mem0, A_Wq, A_Wk, A_Wv, A_Wo, A_seg_len, B_x, B_w_gate, B_w_up, B_w_down, c, w_mod):

    def _A_ref(x, mem0, Wq, Wk, Wv, Wo, seg_len):
        B, S, Dm = x.shape
        H, Dh = (Wq.shape[1], Wq.shape[2])
        M = mem0.shape[1]
        scale = Dh ** (-0.5)
        n_seg = S // seg_len
        L = M + seg_len
        idx = torch.arange(L)
        mask = (idx[:, None] < M) | (idx[:, None] >= idx[None, :])
        mem = mem0
        outs = []
        for s in range(n_seg):
            xs = x[:, s * seg_len:(s + 1) * seg_len, :].clone()
            h = torch.concatenate([mem, xs], axis=1)
            q = torch.einsum('btd,dhe->bhte', h, Wq)
            k = torch.einsum('btd,dhe->bhte', h, Wk)
            v = torch.einsum('btd,dhe->bhte', h, Wv)
            scores = torch.einsum('bhqe,bhke->bhqk', q, k) * scale
            scores = torch.where(mask[None, None], scores, -1000000000.0)
            p = _softmax(scores, axis=-1)
            o = torch.einsum('bhqk,bhke->bhqe', p, v)
            o = torch.einsum('bhte,hed->btd', o, Wo)
            mem = o[:, :M, :].clone()
            outs.append(o[:, M:, :])
        return torch.concatenate(outs, axis=1)

    def _B_ref(x, w_gate, w_up, w_down):
        g = torch.einsum('bsd,df->bsf', x, w_gate)
        u = torch.einsum('bsd,df->bsf', x, w_up)
        h = _gelu(g) * u
        return torch.einsum('bsf,fd->bsd', h, w_down)
    x = A_x
    mod = (_silu(c) @ w_mod).reshape(c.shape[0], 6, 1, -1)
    sa, ca, ga, sb, cb, gb = (mod[:, 0].clone(), mod[:, 1].clone(), mod[:, 2].clone(), mod[:, 3].clone(), mod[:, 4].clone(), mod[:, 5].clone())
    h = _layernorm(x, axis=-1) * (1 + ca) + sa
    x = x + ga * _A_ref(h, A_mem0, A_Wq, A_Wk, A_Wv, A_Wo, A_seg_len)
    h = _layernorm(x, axis=-1) * (1 + cb) + sb
    x = x + gb * _B_ref(h, B_w_gate, B_w_up, B_w_down)
    return x
```

```cpp
#include <hip/hip_runtime.h>
#include <hip/hip_bf16.h>

#define DEV __device__ __forceinline__
typedef unsigned int u32;
typedef unsigned long long u64;
typedef __attribute__((ext_vector_type(2))) __bf16 bf2;
typedef __attribute__((ext_vector_type(8))) __bf16 bf8;
typedef __attribute__((ext_vector_type(16))) __bf16 bf16v;
typedef __attribute__((ext_vector_type(4))) short s4;
typedef __attribute__((ext_vector_type(2))) float f2;
typedef __attribute__((ext_vector_type(4))) float f4;
typedef __attribute__((ext_vector_type(2))) u32 u2;
typedef __attribute__((ext_vector_type(4))) u32 u4;
typedef __attribute__((ext_vector_type(8))) u32 u8;
typedef __attribute__((address_space(3))) char* ldsp;
typedef const __attribute__((address_space(1))) char* gcp;
typedef __attribute__((address_space(1))) char* gp;

constexpr int S_ = 2048, DM = 512, NH = 32, DH = 128, NSEG = 64, DF = 1376, NFC = 43;

constexpr size_t OFF_CTRL = 0;
constexpr size_t OFF_MOD = 65536;
constexpr size_t OFF_H = 1u << 20;
constexpr size_t OFF_X1 = 3u << 20;
constexpr size_t OFF_H2 = 5u << 20;
constexpr size_t OFF_ACT = 7u << 20;
constexpr size_t OFF_KV = 13u << 20;
constexpr size_t OFF_KVM = 45u << 20;
constexpr size_t OFF_P2 = 61u << 20;
constexpr size_t OFF_CH = 126u << 20;

DEV float asf(u32 x) { return __builtin_bit_cast(float, x); }
DEV u32 asu(float x) { return __builtin_bit_cast(u32, x); }
DEV u32 cvtpk(float a, float b) {
  f2 v = {a, b};
  bf2 r = __builtin_convertvector(v, bf2);
  return __builtin_bit_cast(u32, r);
}
DEV float bflo(u32 w) { return asf(w << 16); }
DEV float bfhi(u32 w) { return asf(w & 0xffff0000u); }
DEV float bfr(float x) { return bflo(cvtpk(x, 0.f)); }
DEV f4 mfma32(u4 a, u4 b, f4 c) {
  return __builtin_amdgcn_mfma_f32_16x16x32_bf16(__builtin_bit_cast(bf8, a), __builtin_bit_cast(bf8, b), c, 0, 0, 0);
}
DEV f4 mfma16(u2 a, u2 b, f4 c) {
  return __builtin_amdgcn_mfma_f32_16x16x16bf16_1k(__builtin_bit_cast(s4, a), __builtin_bit_cast(s4, b), c, 0, 0, 0);
}
DEV f4 smfmac(u4 a, u8 b, f4 c, int idx) {
  return __builtin_amdgcn_smfmac_f32_16x16x64_bf16(__builtin_bit_cast(bf8, a), __builtin_bit_cast(bf16v, b), c, idx, 0, 0);
}
DEV u2 trread(ldsp p) {
  return __builtin_bit_cast(u2, __builtin_amdgcn_ds_read_tr16_b64_v4i16((__attribute__((address_space(3))) s4*)p));
}
DEV u4 lds_r128(ldsp p) { return *(__attribute__((address_space(3))) u4*)p; }
DEV void lds_w128(ldsp p, u4 v) { *(__attribute__((address_space(3))) u4*)p = v; }
DEV void lds_w32(ldsp p, u32 v) { *(__attribute__((address_space(3))) u32*)p = v; }
DEV f4 lds_rf4(ldsp p) { return *(__attribute__((address_space(3))) f4*)p; }
DEV void lds_wf4(ldsp p, f4 v) { *(__attribute__((address_space(3))) f4*)p = v; }
DEV void glds16(const void* g, u32 ldsaddr) {
  u32 keep;
  asm volatile("s_mov_b32 %0, m0\n\ts_mov_b32 m0, %2\n\ts_nop 0\n\tglobal_load_lds_dwordx4 %1, off\n\ts_mov_b32 m0, %0"
               : "=&s"(keep) : "v"(g), "s"(ldsaddr) : "memory");
}
DEV void glds16_sc1(const void* g, u32 ldsaddr) {
  u32 keep;
  asm volatile("s_mov_b32 %0, m0\n\ts_mov_b32 m0, %2\n\ts_nop 0\n\tglobal_load_lds_dwordx4 %1, off sc1\n\ts_mov_b32 m0, %0"
               : "=&s"(keep) : "v"(g), "s"(ldsaddr) : "memory");
}
DEV void wait_vm0() { asm volatile("s_waitcnt vmcnt(0)" ::: "memory"); }
DEV void wait_lgkm0() { asm volatile("s_waitcnt lgkmcnt(0)" ::: "memory"); }
DEV void bar() { asm volatile("s_waitcnt lgkmcnt(0)\n\ts_barrier" ::: "memory"); }
DEV u32 rfl(u32 x) { return (u32)__builtin_amdgcn_readfirstlane((int)x); }
typedef __amdgpu_buffer_rsrc_t rsrc_t;
DEV rsrc_t mkrsrc(const void* p, u32 bytes) { return __builtin_amdgcn_make_buffer_rsrc((void*)p, (short)0, (int)bytes, 0x00020000); }
constexpr int AUX_SC1 = 16;
DEV u4 bload128(rsrc_t r, u32 off) { return __builtin_amdgcn_raw_buffer_load_b128(r, off, 0, AUX_SC1); }
DEV void refresh() { asm volatile("" ::: "memory"); }
DEV u4 bpoll128(rsrc_t r, u32 off) { return __builtin_amdgcn_raw_buffer_load_b128(r, off, 0, AUX_SC1); }
DEV u2 bload64(rsrc_t r, u32 off) { return __builtin_amdgcn_raw_buffer_load_b64(r, off, 0, AUX_SC1); }
DEV void bstore128(rsrc_t r, u32 off, u4 v) { __builtin_amdgcn_raw_buffer_store_b128(v, r, off, 0, AUX_SC1); }
DEV void bstore64(rsrc_t r, u32 off, u2 v) { __builtin_amdgcn_raw_buffer_store_b64(v, r, off, 0, AUX_SC1); }
DEV void bstore32(rsrc_t r, u32 off, u32 v) { __builtin_amdgcn_raw_buffer_store_b32(v, r, off, 0, AUX_SC1); }
DEV float swap32(float x) {
  auto r = __builtin_amdgcn_permlane32_swap(asu(x), asu(x), false, false);
  int l = __lane_id();
  return asf(l < 32 ? r[1] : r[0]);
}
DEV float red4_max(float x) {
  auto a = __builtin_amdgcn_permlane32_swap(asu(x), asu(x), false, false);
  x = fmaxf(asf(a[0]), asf(a[1]));
  auto b = __builtin_amdgcn_permlane16_swap(asu(x), asu(x), false, false);
  return fmaxf(asf(b[0]), asf(b[1]));
}
DEV float red4_sum(float x) {
  auto a = __builtin_amdgcn_permlane32_swap(asu(x), asu(x), false, false);
  x = asf(a[0]) + asf(a[1]);
  auto b = __builtin_amdgcn_permlane16_swap(asu(x), asu(x), false, false);
  return asf(b[0]) + asf(b[1]);
}
DEV float wave_sum(float x) {
#pragma unroll
  for (int o = 1; o < 16; o <<= 1) x += __shfl_xor(x, o);
  return red4_sum(x);
}

DEV int swz256(int r) { return (r & 1) | ((((r >> 1) ^ (r >> 2)) & 1) << 1) | (((r >> 3) & 1) << 2); }
template <int NROWS>
DEV void stage_block(const __hip_bfloat16* src, int ld, u32 ldsoff, int w, int l) {
  constexpr int PER = NROWS / 16;
#pragma unroll
  for (int i = 0; i < PER; ++i) {
    int row = (w * PER + i) * 4 + (l >> 4);
    int ch = (l & 15) ^ (swz256(row) << 1);
    glds16((const char*)src + (size_t)row * ld * 2 + ch * 16, ldsoff + (u32)(w * PER + i) * 1024u);
  }
}
DEV u32 blk_addr(int row, int col) { return (u32)(row * 256 + (((col >> 4) ^ swz256(row)) << 5) + (col & 15) * 2); }
DEV u4 dense_frag(ldsp blk, int k0, int cb, int l) {
  int q = (l >> 2) & 3, g = l >> 4;
  u2 a = trread(blk + blk_addr(k0 + 8 * g + q, cb));
  u2 b = trread(blk + blk_addr(k0 + 8 * g + 4 + q, cb));
  return (u4){a.x, a.y, b.x, b.y};
}
DEV u8 sparse_frag(ldsp blk, int k0, int cb, int l) {
  int q = (l >> 2) & 3, g = l >> 4;
  int b = k0 + 16 * (g >> 1) + 4 * (g & 1) + (q & 1) + 8 * (q >> 1);
  u2 r0 = trread(blk + blk_addr(b, cb));
  u2 r1 = trread(blk + blk_addr(b + 2, cb));
  u2 r2 = trread(blk + blk_addr(b + 32, cb));
  u2 r3 = trread(blk + blk_addr(b + 34, cb));
  return (u8){r0.x, r0.y, r1.x, r1.y, r2.x, r2.y, r3.x, r3.y};
}

struct Params {
  const __hip_bfloat16 *x, *mem0, *Wq, *Wk, *Wv, *Wo, *wg, *wu, *wd, *c, *wmod;
  __hip_bfloat16* out;
  char* ws;
};

DEV void role_mod(const Params& P, int job, char* smem) {
  const int t = threadIdx.x;
  const int cc = t & 3, kg = t >> 2;
  float acc[8] = {0, 0, 0, 0, 0, 0, 0, 0};
  u4 wv[8];
#pragma unroll
  for (int i = 0; i < 8; ++i) wv[i] = *(const u4*)(P.wmod + (size_t)(kg * 8 + i) * 3072 + job * 32 + cc * 8);
  u4 cv = *(const u4*)(P.c + kg * 8);
#pragma unroll
  for (int i = 0; i < 8; ++i) {
    u32 cw = cv[i >> 1];
    float c = (i & 1) ? bfhi(cw) : bflo(cw);
    float sc = bfr(c / (1.f + __expf(-c)));
#pragma unroll
    for (int j = 0; j < 4; ++j) { acc[2 * j] += sc * bflo(wv[i][j]); acc[2 * j + 1] += sc * bfhi(wv[i][j]); }
  }
#pragma unroll
  for (int j = 0; j < 8; ++j)
#pragma unroll
    for (int o = 4; o < 64; o <<= 1) acc[j] += __shfl_xor(acc[j], o);
  float* red = (float*)smem;
  if ((t & 63) < 4)
#pragma unroll
    for (int j = 0; j < 8; ++j) red[(t >> 6) * 32 + cc * 8 + j] = acc[j];
  __syncthreads();
  if (t < 32) {
    float v = bfr(red[t] + red[32 + t] + red[64 + t] + red[96 + t]);
    rsrc_t r = mkrsrc(P.ws + OFF_MOD, 3072 * 4);
    bstore32(r, (job * 32 + t) * 4, asu(v));
  }
  __syncthreads();
}

DEV void ln_row_out(const float* v, const float* sc1p, const float* sh, u4& o) {}
DEV void role_ln1_row(const Params& P, int row, int l) {
  u4 xv = *(const u4*)(P.x + (size_t)row * DM + l * 8);
  float v[8];
#pragma unroll
  for (int j = 0; j < 4; ++j) { v[2 * j] = bflo(xv[j]); v[2 * j + 1] = bfhi(xv[j]); }
  float s = 0;
#pragma unroll
  for (int j = 0; j < 8; ++j) s += v[j];
  float mean = wave_sum(s) * (1.f / DM);
  float q = 0;
#pragma unroll
  for (int j = 0; j < 8; ++j) { v[j] -= mean; q += v[j] * v[j]; }
  float rstd = rsqrtf(wave_sum(q) * (1.f / DM) + 1e-5f);
  rsrc_t rm = mkrsrc(P.ws + OFF_MOD, 3072 * 4);
  u4 sa0 = bload128(rm, l * 32), sa1 = bload128(rm, l * 32 + 16);
  u4 ca0 = bload128(rm, 2048 + l * 32), ca1 = bload128(rm, 2048 + l * 32 + 16);
  float h[8];
#pragma unroll
  for (int j = 0; j < 8; ++j) {
    float sa = asf(j < 4 ? sa0[j & 3] : sa1[j & 3]);
    float ca = asf(j < 4 ? ca0[j & 3] : ca1[j & 3]);
    h[j] = v[j] * rstd * bfr(1.f + ca) + sa;
  }
  u4 o = {cvtpk(h[0], h[1]), cvtpk(h[2], h[3]), cvtpk(h[4], h[5]), cvtpk(h[6], h[7])};
  rsrc_t rh = mkrsrc(P.ws + OFF_H, S_ * DM * 2);
  bstore128(rh, (row * DM + l * 8) * 2, o);
}

template <bool PERM>
DEV void stage_rows(const char* base  , u32 ldsoff, int w, int l) {
  const int r = l & 15, g = l >> 4;
#pragma unroll
  for (int i = 0; i < 8; ++i) {
    int f = w * 8 + i, tt = f >> 4, kk = f & 15;
    int tok = PERM ? (8 * (r >> 2) + 4 * tt + (r & 3)) : (16 * tt + r);
    glds16_sc1(base + tok * 1024 + kk * 64 + g * 16, ldsoff + (u32)f * 1024u);
  }
}

DEV void role_kv(const Params& P, int h, int s0, int ds, ldsp lds) {
  const int t = threadIdx.x, l = t & 63, w = rfl(t >> 6);
  const int g = l >> 4;
  u4 W[4][16];
  const __hip_bfloat16* Wk = P.Wk + h * DH;
  const __hip_bfloat16* Wv = P.Wv + h * DH;
  stage_block<128>(Wk, NH * DH, 0, w, l);
  stage_block<128>(Wv, NH * DH, 32768, w, l);
#pragma unroll
  for (int u = 0; u < 4; ++u) {
    if (u < 3) {
      stage_block<128>(Wk + (size_t)(u + 1) * 128 * NH * DH, NH * DH, ((u + 1) & 1) * 65536, w, l);
      stage_block<128>(Wv + (size_t)(u + 1) * 128 * NH * DH, NH * DH, ((u + 1) & 1) * 65536 + 32768, w, l);
      asm volatile("s_waitcnt vmcnt(16)" ::: "memory");
    } else wait_vm0();
    bar();
    ldsp blk = lds + (u & 1) * 65536 + (w >> 1) * 32768;
#pragma unroll
    for (int pt = 0; pt < 4; ++pt)
#pragma unroll
      for (int k4 = 0; k4 < 4; ++k4) {
        int p = (w & 1) * 2 + (pt >> 1), tt = pt & 1;
        int cb = (w < 2) ? 32 * p + 16 * tt + 4 * (l & 3) : 32 * p + 8 * (l & 3) + 4 * tt;
        W[pt][u * 4 + k4] = dense_frag(blk, k4 * 32, cb, l);
      }
    bar();
  }
  const char* H = P.ws + OFF_H;
  rsrc_t rkv = mkrsrc(P.ws + OFF_KV, 0x2000000);
  int buf = 0;
  if (s0 < NSEG) stage_rows<true>(H + (size_t)s0 * 32768, 0, w, l);
  for (int s = s0; s < NSEG; s += ds) {
    wait_vm0();
    bar();
    if (s + ds < NSEG) stage_rows<true>(H + (size_t)(s + ds) * 32768, (buf ^ 1) * 32768, w, l);
    ldsp hb = lds + buf * 32768 + l * 16;
    f4 acc[4][2];
#pragma unroll
    for (int pt = 0; pt < 4; ++pt) { acc[pt][0] = (f4){0, 0, 0, 0}; acc[pt][1] = (f4){0, 0, 0, 0}; }
    if (w < 2) {
#pragma unroll
      for (int kk = 0; kk < 16; ++kk) {
        u4 h0 = lds_r128(hb + kk * 1024), h1 = lds_r128(hb + (16 + kk) * 1024);
#pragma unroll
        for (int pt = 0; pt < 4; ++pt) { acc[pt][0] = mfma32(W[pt][kk], h0, acc[pt][0]); acc[pt][1] = mfma32(W[pt][kk], h1, acc[pt][1]); }
      }
#pragma unroll
      for (int pp = 0; pp < 2; ++pp)
#pragma unroll
        for (int tt = 0; tt < 2; ++tt) {
          f4 a = acc[2 * pp][tt], b = acc[2 * pp + 1][tt];
          u4 o = {cvtpk(a[0], b[0]), cvtpk(a[1], b[1]), cvtpk(a[2], b[2]), cvtpk(a[3], b[3])};
          int p = w * 2 + pp;
          bstore128(rkv, (u32)(s * NH + h) * 16384u + (tt * 4 + p) * 1024 + l * 16, o);
        }
    } else {
#pragma unroll
      for (int kk = 0; kk < 16; ++kk) {
        u4 h0 = lds_r128(hb + kk * 1024), h1 = lds_r128(hb + (16 + kk) * 1024);
#pragma unroll
        for (int pt = 0; pt < 4; ++pt) { acc[pt][0] = mfma32(h0, W[pt][kk], acc[pt][0]); acc[pt][1] = mfma32(h1, W[pt][kk], acc[pt][1]); }
      }
#pragma unroll
      for (int pt = 0; pt < 4; ++pt) {
        f4 a = acc[pt][0], b = acc[pt][1];
        u4 o = {cvtpk(a[0], a[1]), cvtpk(a[2], a[3]), cvtpk(b[0], b[1]), cvtpk(b[2], b[3])};
        int et = (w - 2) * 4 + pt;
        bstore128(rkv, (u32)(s * NH + h) * 16384u + 8192 + et * 1024 + l * 16, o);
      }
    }
    buf ^= 1;
  }
  wait_vm0();
  bar();
  (void)g;
}

constexpr u32 CL_WV = 0, CL_M = 131072, CL_KV = 139264, CL_QK = 155648, CL_O = 160000;
constexpr u32 SENT = 0xffffffffu;
constexpr int SPIN_MAX = 200000;
DEV void fold2(f4 x, f4 y, float* o) {
#pragma unroll
  for (int c = 0; c < 4; ++c) {
    auto r = __builtin_amdgcn_permlane32_swap(asu(x[c]), asu(y[c]), false, false);
    o[c] = asf(r[0]) + asf(r[1]);
  }
}
DEV void role_chain(const Params& P, int h, ldsp lds, char* chbase) {
  const int t = threadIdx.x, l = t & 63, w = rfl(t >> 6);
  const int rho = l & 15, g = l >> 4;
  u8 Wq[2][8], Wk[2][8], Wo[8][2];
  const int LD = NH * DH;
  {
    const __hip_bfloat16* q = P.Wq + h * DH; const __hip_bfloat16* k = P.Wk + h * DH; const __hip_bfloat16* v = P.Wv + h * DH;
    const __hip_bfloat16* o = P.Wo + (size_t)h * DH * DM;
    auto issue = [&](int u) {
      u32 off = (u & 1) * 65536;
      if (u < 2) stage_block<256>(q + (size_t)u * 256 * LD, LD, off, w, l);
      else if (u < 4) stage_block<256>(k + (size_t)(u - 2) * 256 * LD, LD, off, w, l);
      else if (u < 6) {
#pragma unroll
        for (int b = 0; b < 4; ++b) stage_block<64>(o + (size_t)(u - 4) * 64 * DM + b * 128, DM, off + b * 16384, w, l);
      } else stage_block<256>(v + (size_t)(u - 6) * 256 * LD, LD, off, w, l);
    };
    issue(0);
#pragma unroll
    for (int u = 0; u < 8; ++u) {
      if (u < 7) { issue(u + 1); asm volatile("s_waitcnt vmcnt(16)" ::: "memory"); } else wait_vm0();
      bar();
      ldsp blk = lds + (u & 1) * 65536;
      if (u < 4) {
#pragma unroll
        for (int tt = 0; tt < 2; ++tt)
#pragma unroll
          for (int k4 = 0; k4 < 4; ++k4) {
            u8 f = sparse_frag(blk, k4 * 64, 32 * w + 16 * tt + 4 * (l & 3), l);
            if (u < 2) Wq[tt][u * 4 + k4] = f; else Wk[tt][(u - 2) * 4 + k4] = f;
          }
      } else if (u < 6) {
#pragma unroll
        for (int T = 0; T < 8; ++T) Wo[T][u - 4] = sparse_frag(blk + w * 16384, 0, 16 * T + 4 * (l & 3), l);
      } else {
        u8 f[2][4];
#pragma unroll
        for (int tt = 0; tt < 2; ++tt)
#pragma unroll
          for (int k4 = 0; k4 < 4; ++k4) f[tt][k4] = sparse_frag(blk, k4 * 64, 32 * w + 8 * (l & 3) + 4 * tt, l);
        bar();
#pragma unroll
        for (int tt = 0; tt < 2; ++tt)
#pragma unroll
          for (int k4 = 0; k4 < 4; ++k4) {
            ldsp d = blk + k4 * 16384 + w * 4096 + tt * 2048 + l * 16;
            lds_w128(d, (u4){f[tt][k4][0], f[tt][k4][1], f[tt][k4][2], f[tt][k4][3]});
            lds_w128(d + 1024, (u4){f[tt][k4][4], f[tt][k4][5], f[tt][k4][6], f[tt][k4][7]});
          }
      }
      bar();
    }
  }
  const int idx = (rho < 8) ? 0x4444 : 0xEEEE;
  u4 sel;
#pragma unroll
  for (int j = 0; j < 4; ++j) sel[j] = ((rho & 7) == 2 * j ? 0x3f80u : 0u) | ((rho & 7) == 2 * j + 1 ? 0x3f800000u : 0u);
  rsrc_t rkv = mkrsrc(P.ws + OFF_KV, 0x2000000);
  rsrc_t rkvm = mkrsrc(P.ws + OFF_KVM, 0x1000000);
  rsrc_t rch = mkrsrc(chbase, 0x200000);
  const char* KV = P.ws + OFF_KV;
  {
    const char* m0 = (const char*)P.mem0;
#pragma unroll
    for (int i = 0; i < 2; ++i) {
      int kap = 2 * w + i;
      u4 v = *(const u4*)(m0 + (rho & 7) * 1024 + (64 * kap + 16 * g + 8 * (rho >> 3)) * 2);
      lds_w128(lds + CL_M + kap * 1024 + l * 16, v);
    }
  }
  auto stage_kv = [&](int s) {
#pragma unroll
    for (int i = 0; i < 4; ++i) glds16_sc1(KV + (size_t)(s * NH + h) * 16384 + (w * 4 + i) * 1024 + l * 16, CL_KV + (u32)(w * 4 + i) * 1024u);
  };
  stage_kv(0);
  const float cexp = 0.08838834764831845f * 1.4426950408889634f;
  for (int s = 0; s < NSEG; ++s) {
    bar();
    u4 mf[8];
#pragma unroll
    for (int k = 0; k < 8; ++k) mf[k] = lds_r128(lds + CL_M + k * 1024 + l * 16);
    f4 qa[2] = {{0, 0, 0, 0}, {0, 0, 0, 0}}, ka[2] = {{0, 0, 0, 0}, {0, 0, 0, 0}}, va[2] = {{0, 0, 0, 0}, {0, 0, 0, 0}};
#pragma unroll
    for (int k = 0; k < 8; ++k) {
      qa[0] = smfmac(mf[k], Wq[0][k], qa[0], idx);
      ka[0] = smfmac(mf[k], Wk[0][k], ka[0], idx);
      qa[1] = smfmac(mf[k], Wq[1][k], qa[1], idx);
      ka[1] = smfmac(mf[k], Wk[1][k], ka[1], idx);
    }
#pragma unroll
    for (int k = 0; k < 8; ++k)
#pragma unroll
      for (int tt = 0; tt < 2; ++tt) {
        ldsp d = lds + CL_WV + k * 16384 + w * 4096 + tt * 2048 + l * 16;
        u4 a = lds_r128(d), b = lds_r128(d + 1024);
        va[tt] = smfmac(mf[k], (u8){a[0], a[1], a[2], a[3], b[0], b[1], b[2], b[3]}, va[tt], idx);
      }
    {
      float o0[4], o1[4];
      fold2(qa[0], ka[0], o0);
      fold2(qa[1], ka[1], o1);
      ldsp d = lds + CL_QK + (g >> 1) * 2176 + (4 * (g & 1)) * 272 + (32 * w + 2 * rho) * 2;
#pragma unroll
      for (int c = 0; c < 4; ++c) lds_w32(d + c * 272, cvtpk(o0[c], o1[c]));
    }
    u2 vm[2];
#pragma unroll
    for (int tt = 0; tt < 2; ++tt) vm[tt] = (u2){cvtpk(va[tt][0], va[tt][1]), cvtpk(va[tt][2], va[tt][3])};
    wait_vm0();
    bar();
    {
      u4 kf = lds_r128(lds + CL_QK + 2176 + (rho & 7) * 272 + w * 64 + g * 16);
      u32 base = (u32)(s * NH + h) * 8192u;
      if (rho < 8) bstore128(rkvm, base + w * 512 + (g * 8 + rho) * 16, kf);
#pragma unroll
      for (int tt = 0; tt < 2; ++tt) bstore64(rkvm, base + 2048 + (2 * w + tt) * 512 + l * 8, vm[tt]);
    }
    if (s == NSEG - 1) break;
    f4 st[3] = {{0, 0, 0, 0}, {0, 0, 0, 0}, {0, 0, 0, 0}};
#pragma unroll
    for (int kk = 0; kk < 4; ++kk) {
      u4 qf = lds_r128(lds + CL_QK + (rho & 7) * 272 + kk * 64 + g * 16);
      u4 kf = lds_r128(lds + CL_QK + 2176 + (rho & 7) * 272 + kk * 64 + g * 16);
      u4 k0 = lds_r128(lds + CL_KV + kk * 1024 + l * 16), k1 = lds_r128(lds + CL_KV + (4 + kk) * 1024 + l * 16);
      st[0] = mfma32(k0, qf, st[0]);
      st[1] = mfma32(k1, qf, st[1]);
      st[2] = mfma32(kf, qf, st[2]);
    }
    u4 v0 = lds_r128(lds + CL_KV + 8192 + (2 * w) * 1024 + l * 16), v1 = lds_r128(lds + CL_KV + 8192 + (2 * w + 1) * 1024 + l * 16);
    float mx = st[0][0];
#pragma unroll
    for (int a = 0; a < 3; ++a)
#pragma unroll
      for (int c = 0; c < 4; ++c) mx = fmaxf(mx, st[a][c]);
    mx = red4_max(mx);
    float pr[3][4], sum = 0, summ = 0;
#pragma unroll
    for (int a = 0; a < 3; ++a)
#pragma unroll
      for (int c = 0; c < 4; ++c) {
        pr[a][c] = __builtin_amdgcn_exp2f((st[a][c] - mx) * cexp);
        if (a < 2) sum += pr[a][c]; else summ += pr[a][c];
      }
    sum += (g < 2) ? summ : 0.f;
    u4 pt = {cvtpk(pr[0][0], pr[0][1]), cvtpk(pr[0][2], pr[0][3]), cvtpk(pr[1][0], pr[1][1]), cvtpk(pr[1][2], pr[1][3])};
    u2 pm = {cvtpk(pr[2][0], pr[2][1]), cvtpk(pr[2][2], pr[2][3])};
    f4 oa = mfma32(v0, pt, (f4){0, 0, 0, 0}), ob = mfma32(v1, pt, (f4){0, 0, 0, 0});
    oa = mfma32((u4){vm[0].x, vm[0].y, 0u, 0u}, (u4){pm.x, pm.y, 0u, 0u}, oa);
    ob = mfma32((u4){vm[1].x, vm[1].y, 0u, 0u}, (u4){pm.x, pm.y, 0u, 0u}, ob);
    float inv = 1.f / red4_sum(sum);
    {
      u4 o = {cvtpk(oa[0] * inv, oa[1] * inv), cvtpk(oa[2] * inv, oa[3] * inv), cvtpk(ob[0] * inv, ob[1] * inv), cvtpk(ob[2] * inv, ob[3] * inv)};
      if (rho < 8) lds_w128(lds + CL_O + (w >> 1) * 1024 + ((2 * (w & 1) + (g >> 1)) * 16 + (g & 1) * 8 + rho) * 16, o);
    }
    bar();
    stage_kv(s + 1);
    u4 of0 = lds_r128(lds + CL_O + l * 16), of1 = lds_r128(lds + CL_O + 1024 + l * 16);
    const u32 slot = (u32)(s & 1) * 0x80000u;
#pragma unroll
    for (int p = 0; p < 4; ++p) {
      f4 x = smfmac(of0, Wo[2 * p][0], (f4){0, 0, 0, 0}, idx);
      f4 y = smfmac(of0, Wo[2 * p + 1][0], (f4){0, 0, 0, 0}, idx);
      x = smfmac(of1, Wo[2 * p][1], x, idx);
      y = smfmac(of1, Wo[2 * p + 1][1], y, idx);
      float o[4];
      fold2(x, y, o);
      u2 pk = {min(cvtpk(o[0], o[1]), SENT - 1), cvtpk(o[2], o[3])};
      int T = 8 * w + 2 * p + (l >> 5);
      bstore64(rch, slot + (u32)T * 16384u + h * 256 + rho * 16 + (g & 1) * 8, pk);
    }
    const u32 moff = 0x100000u + (u32)(s + 1) * 16384u;
    if (w == 0) {
      u4 d[8];
      const u32 base = slot + (u32)h * 16384u + l * 16;
      for (int it = 0;; ++it) {
        if (it > SPIN_MAX) return;
        refresh();
        u32 m = 0;
#pragma unroll
        for (int u = 0; u < 8; ++u) d[u] = bpoll128(rch, base + u * 1024);
#pragma unroll
        for (int u = 0; u < 8; ++u) m = max(m, max(d[u][0], d[u][2]));
        if (__builtin_amdgcn_ballot_w64(m == SENT) == 0) break;
      }
      f4 acc = {0, 0, 0, 0};
#pragma unroll
      for (int u = 0; u < 8; ++u) acc = mfma32(d[u], sel, acc);
      u2 pk = {min(cvtpk(acc[0], acc[1]), SENT - 1), cvtpk(acc[2], acc[3])};
      if (rho < 8) bstore64(rch, moff + (2 * h + (g >> 1)) * 128 + rho * 16 + (g & 1) * 8, pk);
      u4 pz = {SENT, SENT, SENT, SENT};
#pragma unroll
      for (int u = 0; u < 8; ++u) bstore128(rch, base + u * 1024, pz);
    }
    {
      u4 d0, d1;
      const u32 base = moff + w * 2048 + l * 16;
      for (int it = 0;; ++it) {
        if (it > SPIN_MAX) return;
        refresh();
        d0 = bpoll128(rch, base);
        d1 = bpoll128(rch, base + 1024);
        u32 m = max(max(d0[0], d0[2]), max(d1[0], d1[2]));
        if (__builtin_amdgcn_ballot_w64(m == SENT) == 0) break;
      }
      lds_w128(lds + CL_M + (2 * w) * 1024 + l * 16, d0);
      lds_w128(lds + CL_M + (2 * w + 1) * 1024 + l * 16, d1);
    }
  }
  wait_vm0();
}

constexpr u32 AL_H = 0  , AL_KV = 65536  , AL_KVM = 81920  , AL_Q = 90112  , AL_O = 98304  ;
DEV void role_att(const Params& P, int h, int s0, int ds, ldsp lds) {
  const int t = threadIdx.x, l = t & 63, w = rfl(t >> 6);
  const int rho = l & 15, g = l >> 4;
  u4 Wq[2][16], Wo[8][4];
  const int LD = NH * DH;
  {
    const __hip_bfloat16* q = P.Wq + h * DH;
    const __hip_bfloat16* o = P.Wo + (size_t)h * DH * DM;
    auto issue = [&](int u) {
      u32 off = (u & 1) * 65536;
      if (u < 2) stage_block<256>(q + (size_t)u * 256 * LD, LD, off, w, l);
      else {
#pragma unroll
        for (int b = 0; b < 4; ++b) stage_block<64>(o + (size_t)(u - 2) * 64 * DM + b * 128, DM, off + b * 16384, w, l);
      }
    };
    issue(0);
#pragma unroll
    for (int u = 0; u < 4; ++u) {
      if (u < 3) { issue(u + 1); asm volatile("s_waitcnt vmcnt(16)" ::: "memory"); } else wait_vm0();
      bar();
      ldsp blk = lds + (u & 1) * 65536;
      if (u < 2) {
#pragma unroll
        for (int tt = 0; tt < 2; ++tt)
#pragma unroll
          for (int k8 = 0; k8 < 8; ++k8) Wq[tt][u * 8 + k8] = dense_frag(blk, k8 * 32, 32 * w + 16 * tt + 4 * (l & 3), l);
      } else {
#pragma unroll
        for (int T = 0; T < 8; ++T)
#pragma unroll
          for (int k2 = 0; k2 < 2; ++k2) Wo[T][(u - 2) * 2 + k2] = dense_frag(blk + w * 16384, k2 * 32, 32 * (T >> 1) + 8 * (l & 3) + 4 * (T & 1), l);
      }
      bar();
    }
  }
  const char* H = P.ws + OFF_H;
  const char* KV = P.ws + OFF_KV;
  const char* KVM = P.ws + OFF_KVM;
  rsrc_t rp2 = mkrsrc(P.ws + OFF_P2, 0x4000000);
  const float cexp = 0.08838834764831845f * 1.4426950408889634f;
  const int qt = w & 1, eh = w >> 1;
  int buf = 0;
  if (s0 < NSEG) stage_rows<true>(H + (size_t)s0 * 32768, AL_H, w, l);
  for (int s = s0; s < NSEG; s += ds) {
    wait_vm0();
    bar();
#pragma unroll
    for (int i = 0; i < 4; ++i) glds16_sc1(KV + (size_t)(s * NH + h) * 16384 + (w * 4 + i) * 1024 + l * 16, AL_KV + (u32)(w * 4 + i) * 1024u);
#pragma unroll
    for (int i = 0; i < 2; ++i) {
      int f = w * 2 + i; if (f > 5) f = 5;
      glds16_sc1(KVM + (size_t)(s * NH + h) * 8192 + f * 1024 + l * 16, AL_KVM + (u32)f * 1024u);
    }
    if (s + ds < NSEG) stage_rows<true>(H + (size_t)(s + ds) * 32768, AL_H + (buf ^ 1) * 32768, w, l);
    asm volatile("" ::: "memory");
    ldsp hb = lds + AL_H + buf * 32768 + l * 16;
    f4 qa[2][2] = {{{0, 0, 0, 0}, {0, 0, 0, 0}}, {{0, 0, 0, 0}, {0, 0, 0, 0}}};
#pragma unroll
    for (int kk = 0; kk < 16; ++kk) {
      u4 h0 = lds_r128(hb + kk * 1024), h1 = lds_r128(hb + (16 + kk) * 1024);
#pragma unroll
      for (int tt = 0; tt < 2; ++tt) { qa[tt][0] = mfma32(Wq[tt][kk], h0, qa[tt][0]); qa[tt][1] = mfma32(Wq[tt][kk], h1, qa[tt][1]); }
    }
#pragma unroll
    for (int q2 = 0; q2 < 2; ++q2) {
      f4 a = qa[0][q2], b = qa[1][q2];
      lds_w128(lds + AL_Q + (q2 * 4 + w) * 1024 + l * 16, (u4){cvtpk(a[0], b[0]), cvtpk(a[1], b[1]), cvtpk(a[2], b[2]), cvtpk(a[3], b[3])});
    }
    asm volatile("s_waitcnt vmcnt(8)" ::: "memory");
    if (s + ds >= NSEG) wait_vm0();
    bar();
    f4 st[3] = {{0, 0, 0, 0}, {0, 0, 0, 0}, {0, 0, 0, 0}};
#pragma unroll
    for (int kk = 0; kk < 4; ++kk) {
      u4 qf = lds_r128(lds + AL_Q + (qt * 4 + kk) * 1024 + l * 16);
      u4 k0 = lds_r128(lds + AL_KV + kk * 1024 + l * 16), k1 = lds_r128(lds + AL_KV + (4 + kk) * 1024 + l * 16);
      u4 kf = lds_r128(lds + AL_KVM + kk * 512 + (g * 8 + (rho & 7)) * 16);
      st[0] = mfma32(k0, qf, st[0]);
      st[1] = mfma32(k1, qf, st[1]);
      st[2] = mfma32(kf, qf, st[2]);
    }
    const int qtok = 8 * (rho >> 2) + 4 * qt + (rho & 3);
    float sc[3][4];
    float mx = -3e38f;
#pragma unroll
    for (int a = 0; a < 3; ++a)
#pragma unroll
      for (int c = 0; c < 4; ++c) {
        float v = st[a][c] * 0.08838834764831845f;
        if (a < 2 && (8 * g + 4 * a + c) > qtok) v = -1e9f;
        sc[a][c] = v;
        mx = fmaxf(mx, v);
      }
    mx = red4_max(mx);
    float pr[3][4], sum = 0, summ = 0;
#pragma unroll
    for (int a = 0; a < 3; ++a)
#pragma unroll
      for (int c = 0; c < 4; ++c) {
        pr[a][c] = __builtin_amdgcn_exp2f((sc[a][c] - mx) * 1.4426950408889634f);
        if (a < 2) sum += pr[a][c]; else summ += pr[a][c];
      }
    sum += (g < 2) ? summ : 0.f;
    float inv = 1.f / red4_sum(sum);
    u4 pt = {cvtpk(pr[0][0], pr[0][1]), cvtpk(pr[0][2], pr[0][3]), cvtpk(pr[1][0], pr[1][1]), cvtpk(pr[1][2], pr[1][3])};
    u2 pm = {cvtpk(pr[2][0], pr[2][1]), cvtpk(pr[2][2], pr[2][3])};
    f4 oa[4];
#pragma unroll
    for (int e4 = 0; e4 < 4; ++e4) {
      int et = eh * 4 + e4;
      u4 vf = lds_r128(lds + AL_KV + 8192 + et * 1024 + l * 16);
      u2 vm = *(__attribute__((address_space(3))) u2*)(lds + AL_KVM + 2048 + et * 512 + l * 8);
      oa[e4] = mfma32(vf, pt, (f4){0, 0, 0, 0});
      oa[e4] = mfma32((u4){vm.x, vm.y, 0u, 0u}, (u4){pm.x, pm.y, 0u, 0u}, oa[e4]);
    }
#pragma unroll
    for (int pp = 0; pp < 2; ++pp) {
      f4 a = oa[2 * pp] * inv, b = oa[2 * pp + 1] * inv;
      lds_w128(lds + AL_O + (qt * 4 + eh * 2 + pp) * 1024 + l * 16, (u4){cvtpk(a[0], a[1]), cvtpk(a[2], a[3]), cvtpk(b[0], b[1]), cvtpk(b[2], b[3])});
    }
    bar();
    u4 of[2][4];
#pragma unroll
    for (int q2 = 0; q2 < 2; ++q2)
#pragma unroll
      for (int kk = 0; kk < 4; ++kk) of[q2][kk] = lds_r128(lds + AL_O + (q2 * 4 + kk) * 1024 + l * 16);
#pragma unroll
    for (int p = 0; p < 4; ++p)
#pragma unroll
      for (int q2 = 0; q2 < 2; ++q2) {
        f4 a = {0, 0, 0, 0}, b = {0, 0, 0, 0};
#pragma unroll
        for (int kk = 0; kk < 4; ++kk) { a = mfma32(Wo[2 * p][kk], of[q2][kk], a); b = mfma32(Wo[2 * p + 1][kk], of[q2][kk], b); }
        int tok = 8 * (rho >> 2) + 4 * q2 + (rho & 3);
        bstore128(rp2, (u32)(s * NH + h) * 32768u + tok * 1024 + (128 * w + 32 * p + 8 * g) * 2,
                  (u4){cvtpk(a[0], a[1]), cvtpk(a[2], a[3]), cvtpk(b[0], b[1]), cvtpk(b[2], b[3])});
      }
    buf ^= 1;
  }
  wait_vm0();
  bar();
  (void)cexp;
}

DEV void job_reduce_row(const Params& P, int row, ldsp lds, u32 loff) {
  const int t = threadIdx.x, l = t & 63, w = t >> 6;
  const int s = row >> 5, tok = row & 31;
  rsrc_t rp2 = mkrsrc(P.ws + OFF_P2, 0x4000000);
  float a[8] = {0, 0, 0, 0, 0, 0, 0, 0};
  u4 d[8];
#pragma unroll
  for (int i = 0; i < 8; ++i) d[i] = bload128(rp2, (u32)(s * NH + w * 8 + i) * 32768u + tok * 1024 + l * 16);
#pragma unroll
  for (int i = 0; i < 8; ++i)
#pragma unroll
    for (int j = 0; j < 4; ++j) { a[2 * j] += bflo(d[i][j]); a[2 * j + 1] += bfhi(d[i][j]); }
  lds_wf4(lds + loff + w * 2048 + l * 32, (f4){a[0], a[1], a[2], a[3]});
  lds_wf4(lds + loff + w * 2048 + l * 32 + 16, (f4){a[4], a[5], a[6], a[7]});
  bar();
  if (w == 0) {
#pragma unroll
    for (int ww = 1; ww < 4; ++ww) {
      f4 x = lds_rf4(lds + loff + ww * 2048 + l * 32), y = lds_rf4(lds + loff + ww * 2048 + l * 32 + 16);
#pragma unroll
      for (int j = 0; j < 4; ++j) { a[j] += x[j]; a[4 + j] += y[j]; }
    }
    rsrc_t rm = mkrsrc(P.ws + OFF_MOD, 3072 * 4);
    u4 xv = *(const u4*)(P.x + (size_t)row * DM + l * 8);
    u4 ga0 = bload128(rm, 4096 + l * 32), ga1 = bload128(rm, 4096 + l * 32 + 16);
    u4 sb0 = bload128(rm, 6144 + l * 32), sb1 = bload128(rm, 6144 + l * 32 + 16);
    u4 cb0 = bload128(rm, 8192 + l * 32), cb1 = bload128(rm, 8192 + l * 32 + 16);
    float v[8];
#pragma unroll
    for (int j = 0; j < 8; ++j) {
      float xx = (j & 1) ? bfhi(xv[j >> 1]) : bflo(xv[j >> 1]);
      float ga = asf(j < 4 ? ga0[j & 3] : ga1[j & 3]);
      v[j] = bfr(xx + bfr(ga * bfr(a[j])));
    }
    u4 x1 = {cvtpk(v[0], v[1]), cvtpk(v[2], v[3]), cvtpk(v[4], v[5]), cvtpk(v[6], v[7])};
    bstore128(mkrsrc(P.ws + OFF_X1, S_ * DM * 2), (row * DM + l * 8) * 2, x1);
    float sm = 0;
#pragma unroll
    for (int j = 0; j < 8; ++j) sm += v[j];
    float mean = wave_sum(sm) * (1.f / DM), q = 0;
#pragma unroll
    for (int j = 0; j < 8; ++j) { v[j] -= mean; q += v[j] * v[j]; }
    float rstd = rsqrtf(wave_sum(q) * (1.f / DM) + 1e-5f);
    float hh[8];
#pragma unroll
    for (int j = 0; j < 8; ++j) {
      float sb = asf(j < 4 ? sb0[j & 3] : sb1[j & 3]);
      float cb = asf(j < 4 ? cb0[j & 3] : cb1[j & 3]);
      hh[j] = v[j] * rstd * bfr(1.f + cb) + sb;
    }
    bstore128(mkrsrc(P.ws + OFF_H2, S_ * DM * 2), (row * DM + l * 8) * 2,
              (u4){cvtpk(hh[0], hh[1]), cvtpk(hh[2], hh[3]), cvtpk(hh[4], hh[5]), cvtpk(hh[6], hh[7])});
  }
  bar();
}

DEV float gelu_tanh(float x) {
  float u = 0.7978845608028654f * (x + 0.044715f * x * x * x);
  float e = __builtin_amdgcn_exp2f(u * 2.8853900817779268f);
  float th = 1.f - 2.f / (e + 1.f);
  return 0.5f * x * (1.f + th);
}
DEV void role_gu(const Params& P, int j, int s0, int ds, ldsp lds) {
  const int t = threadIdx.x, l = t & 63, w = rfl(t >> 6);
  const int fs = w >> 1, tt = w & 1;
  u4 Wg[16], Wu[16];
  {
#pragma unroll
    for (int i = 0; i < 16; ++i) {
      int row = (w * 16 + i) * 8 + (l >> 3);
      int sw = ((row >> 1) & 1) | (((row >> 3) & 1) << 1);
      int ch = (l & 7) ^ (sw << 1);
      const __hip_bfloat16* src = (ch < 4 ? P.wg : P.wu) + (size_t)row * DF + j * 32 + (ch & 3) * 8;
      glds16(src, (u32)(w * 16 + i) * 1024u);
    }
    wait_vm0();
    bar();
    const int q = (l >> 2) & 3, g = l >> 4;
#pragma unroll
    for (int kk = 0; kk < 16; ++kk) {
#pragma unroll
      for (int m = 0; m < 2; ++m) {
        u2 r[2];
#pragma unroll
        for (int hf = 0; hf < 2; ++hf) {
          int row = kk * 32 + 8 * g + 4 * hf + q;
          int sw = ((row >> 1) & 1) | (((row >> 3) & 1) << 1);
          int slot = (m * 2 + fs) ^ sw;
          r[hf] = trread(lds + row * 128 + slot * 32 + (l & 3) * 8);
        }
        if (m == 0) Wg[kk] = (u4){r[0].x, r[0].y, r[1].x, r[1].y}; else Wu[kk] = (u4){r[0].x, r[0].y, r[1].x, r[1].y};
      }
    }
    bar();
  }
  const char* H2 = P.ws + OFF_H2;
  rsrc_t ra = mkrsrc(P.ws + OFF_ACT, NSEG * 2 * NFC * 1024);
  for (int s = s0; s < NSEG; s += ds) {
    stage_rows<false>(H2 + (size_t)s * 32768, 0, w, l);
    wait_vm0();
    bar();
    f4 ga = {0, 0, 0, 0}, ua = {0, 0, 0, 0};
#pragma unroll
    for (int kk = 0; kk < 16; ++kk) {
      u4 hf = lds_r128(lds + (tt * 16 + kk) * 1024 + l * 16);
      ga = mfma32(Wg[kk], hf, ga);
      ua = mfma32(Wu[kk], hf, ua);
    }
    float a[4];
#pragma unroll
    for (int c = 0; c < 4; ++c) a[c] = bfr(gelu_tanh(bfr(ga[c]))) * bfr(ua[c]);
    bstore64(ra, (u32)((s * 2 + tt) * NFC + j) * 1024u + l * 16 + fs * 8, (u2){cvtpk(a[0], a[1]), cvtpk(a[2], a[3])});
    bar();
  }
  wait_vm0();
}

DEV void role_down(const Params& P, int tt, int dq, int s0, int ds, ldsp lds) {
  const int t = threadIdx.x, l = t & 63, w = rfl(t >> 6);
  const int rho = l & 15, g = l >> 4;
  u4 Wd[2][11];
  {
    for (int i = w; i < 86; i += 4) {
      int row = i * 16 + (l >> 2);
      int ch = (l & 3) ^ (((row >> 3) & 1) << 1);
      glds16(P.wd + (size_t)row * DM + dq * 32 + ch * 8, (u32)i * 1024u);
    }
    wait_vm0();
    bar();
    const int q = (l >> 2) & 3, p = l & 3;
#pragma unroll
    for (int i = 0; i < 11; ++i) {
      int j = min(w * 11 + i, NFC - 1);
#pragma unroll
      for (int tl = 0; tl < 2; ++tl) {
        u2 r[2];
#pragma unroll
        for (int hf = 0; hf < 2; ++hf) {
          int row = j * 32 + 16 * hf + 4 * g + q;
          int col = 8 * p + 4 * tl;
          int slot = (col >> 4) ^ ((row >> 3) & 1);
          r[hf] = trread(lds + row * 64 + slot * 32 + (col & 15) * 2);
        }
        u4 f = {r[0].x, r[0].y, r[1].x, r[1].y};
        if (w * 11 + i >= NFC) f = (u4){0, 0, 0, 0};
        Wd[tl][i] = f;
      }
    }
    bar();
  }
  rsrc_t ra = mkrsrc(P.ws + OFF_ACT, NSEG * 2 * NFC * 1024);
  rsrc_t rx1 = mkrsrc(P.ws + OFF_X1, S_ * DM * 2);
  rsrc_t rm = mkrsrc(P.ws + OFF_MOD, 3072 * 4);
  u4 gb0 = bload128(rm, 10240 + (dq * 32 + 8 * g) * 4), gb1 = bload128(rm, 10240 + (dq * 32 + 8 * g) * 4 + 16);
  for (int s = s0; s < NSEG; s += ds) {
    u4 af[11];
#pragma unroll
    for (int i = 0; i < 11; ++i) {
      int j = min(w * 11 + i, NFC - 1);
      af[i] = bload128(ra, (u32)((s * 2 + tt) * NFC + j) * 1024u + l * 16);
    }
    const int row = s * 32 + tt * 16 + rho;
    u4 x1 = bload128(rx1, (row * DM + dq * 32 + 8 * g) * 2);
    f4 a = {0, 0, 0, 0}, b = {0, 0, 0, 0};
#pragma unroll
    for (int i = 0; i < 11; ++i) { a = mfma32(Wd[0][i], af[i], a); b = mfma32(Wd[1][i], af[i], b); }
    lds_wf4(lds + w * 2048 + l * 32, a);
    lds_wf4(lds + w * 2048 + l * 32 + 16, b);
    bar();
    if (w == 0) {
#pragma unroll
      for (int ww = 1; ww < 4; ++ww) { a += lds_rf4(lds + ww * 2048 + l * 32); b += lds_rf4(lds + ww * 2048 + l * 32 + 16); }
      float o[8];
#pragma unroll
      for (int jx = 0; jx < 8; ++jx) {
        float f = jx < 4 ? a[jx & 3] : b[jx & 3];
        float gb = asf(jx < 4 ? gb0[jx & 3] : gb1[jx & 3]);
        float xx = (jx & 1) ? bfhi(x1[jx >> 1]) : bflo(x1[jx >> 1]);
        o[jx] = xx + bfr(gb * bfr(f));
      }
      *(u4*)(P.out + (size_t)row * DM + dq * 32 + 8 * g) = (u4){cvtpk(o[0], o[1]), cvtpk(o[2], o[3]), cvtpk(o[4], o[5]), cvtpk(o[6], o[7])};
    }
    bar();
  }
}

extern __shared__ __attribute__((aligned(16))) char smem[];
DEV char* chain_base(const Params& P) { return (char*)((((size_t)(P.ws + OFF_CH)) + 0x1fffff) & ~(size_t)0x1fffff); }
__global__ void __launch_bounds__(256, 1) k_mod(Params P) { role_mod(P, blockIdx.x, smem); }
__global__ void __launch_bounds__(256, 1) k_ln1(Params P) { role_ln1_row(P, blockIdx.x * 4 + (threadIdx.x >> 6), threadIdx.x & 63); }
__global__ void __launch_bounds__(256, 1) k_kv(Params P) { role_kv(P, blockIdx.x & 31, blockIdx.x >> 5, gridDim.x >> 5, (ldsp)smem); }
__global__ void __launch_bounds__(256, 1) k_chain(Params P) { role_chain(P, blockIdx.x, (ldsp)smem, chain_base(P)); }
__global__ void __launch_bounds__(256, 1) k_att(Params P) { role_att(P, blockIdx.x & 31, blockIdx.x >> 5, gridDim.x >> 5, (ldsp)smem); }
__global__ void __launch_bounds__(256, 1) k_red(Params P) { job_reduce_row(P, blockIdx.x, (ldsp)smem, 0); }
__global__ void __launch_bounds__(256, 1) k_gu(Params P) { role_gu(P, blockIdx.x % NFC, blockIdx.x / NFC, gridDim.x / NFC, (ldsp)smem); }
__global__ void __launch_bounds__(256, 1) k_down(Params P) { role_down(P, blockIdx.x & 1, (blockIdx.x >> 1) & 15, blockIdx.x >> 5, gridDim.x >> 5, (ldsp)smem); }

extern "C" void kernel_launch(void* const* d_in, const int* in_sizes, int n_in, void* d_out, int out_size, void* d_ws,
                              size_t ws_size, hipStream_t stream) {
  Params P;
  P.x = (const __hip_bfloat16*)d_in[0];
  P.mem0 = (const __hip_bfloat16*)d_in[1];
  P.Wq = (const __hip_bfloat16*)d_in[2];
  P.Wk = (const __hip_bfloat16*)d_in[3];
  P.Wv = (const __hip_bfloat16*)d_in[4];
  P.Wo = (const __hip_bfloat16*)d_in[5];
  P.wg = (const __hip_bfloat16*)d_in[8];
  P.wu = (const __hip_bfloat16*)d_in[9];
  P.wd = (const __hip_bfloat16*)d_in[10];
  P.c = (const __hip_bfloat16*)d_in[11];
  P.wmod = (const __hip_bfloat16*)d_in[12];
  P.out = (__hip_bfloat16*)d_out;
  P.ws = (char*)d_ws;
  const int LDSB = 163840;
  (void)hipFuncSetAttribute((const void*)k_kv, hipFuncAttributeMaxDynamicSharedMemorySize, LDSB);
  (void)hipFuncSetAttribute((const void*)k_chain, hipFuncAttributeMaxDynamicSharedMemorySize, LDSB);
  (void)hipFuncSetAttribute((const void*)k_att, hipFuncAttributeMaxDynamicSharedMemorySize, LDSB);
  (void)hipFuncSetAttribute((const void*)k_gu, hipFuncAttributeMaxDynamicSharedMemorySize, LDSB);
  (void)hipFuncSetAttribute((const void*)k_down, hipFuncAttributeMaxDynamicSharedMemorySize, LDSB);
  char* ch = (char*)((((size_t)((char*)d_ws + OFF_CH)) + 0x1fffff) & ~(size_t)0x1fffff);
  (void)hipMemsetAsync(ch, 0xff, 0x200000, stream);
  k_mod<<<96, 256, 1024, stream>>>(P);
  k_ln1<<<512, 256, 0, stream>>>(P);
  k_kv<<<224, 256, LDSB, stream>>>(P);
  k_chain<<<32, 256, LDSB, stream>>>(P);
  k_att<<<256, 256, LDSB, stream>>>(P);
  k_red<<<2048, 256, 8192, stream>>>(P);
  k_gu<<<NFC * 5, 256, LDSB, stream>>>(P);
  k_down<<<256, 256, LDSB, stream>>>(P);
}
```

```cpp
#include <hip/hip_runtime.h>
#include <hip/hip_bf16.h>

#define DEV __device__ __forceinline__
typedef unsigned int u32;
typedef unsigned long long u64;
typedef __attribute__((ext_vector_type(2))) __bf16 bf2;
typedef __attribute__((ext_vector_type(8))) __bf16 bf8;
typedef __attribute__((ext_vector_type(16))) __bf16 bf16v;
typedef __attribute__((ext_vector_type(4))) short s4;
typedef __attribute__((ext_vector_type(2))) float f2;
typedef __attribute__((ext_vector_type(4))) float f4;
typedef __attribute__((ext_vector_type(2))) u32 u2;
typedef __attribute__((ext_vector_type(4))) u32 u4;
typedef __attribute__((ext_vector_type(8))) u32 u8;
typedef __attribute__((address_space(3))) char* ldsp;
typedef const __attribute__((address_space(1))) char* gcp;
typedef __attribute__((address_space(1))) char* gp;

constexpr int S_ = 2048, DM = 512, NH = 32, DH = 128, NSEG = 64, DF = 1376, NFC = 43;

constexpr size_t OFF_CTRL = 0;
constexpr size_t OFF_MOD = 65536;
constexpr size_t OFF_H = 1u << 20;
constexpr size_t OFF_X1 = 3u << 20;
constexpr size_t OFF_H2 = 5u << 20;
constexpr size_t OFF_ACT = 7u << 20;
constexpr size_t OFF_KV = 13u << 20;
constexpr size_t OFF_KVM = 45u << 20;
constexpr size_t OFF_P2 = 61u << 20;
constexpr size_t OFF_CH = 126u << 20;

DEV float asf(u32 x) { return __builtin_bit_cast(float, x); }
DEV u32 asu(float x) { return __builtin_bit_cast(u32, x); }
DEV u32 cvtpk(float a, float b) {
  f2 v = {a, b};
  bf2 r = __builtin_convertvector(v, bf2);
  return __builtin_bit_cast(u32, r);
}
DEV float bflo(u32 w) { return asf(w << 16); }
DEV float bfhi(u32 w) { return asf(w & 0xffff0000u); }
DEV float bfr(float x) { return bflo(cvtpk(x, 0.f)); }
DEV f4 mfma32(u4 a, u4 b, f4 c) {
  return __builtin_amdgcn_mfma_f32_16x16x32_bf16(__builtin_bit_cast(bf8, a), __builtin_bit_cast(bf8, b), c, 0, 0, 0);
}
DEV f4 mfma16(u2 a, u2 b, f4 c) {
  return __builtin_amdgcn_mfma_f32_16x16x16bf16_1k(__builtin_bit_cast(s4, a), __builtin_bit_cast(s4, b), c, 0, 0, 0);
}
DEV f4 smfmac(u4 a, u8 b, f4 c, int idx) {
  return __builtin_amdgcn_smfmac_f32_16x16x64_bf16(__builtin_bit_cast(bf8, a), __builtin_bit_cast(bf16v, b), c, idx, 0, 0);
}
DEV u2 trread(ldsp p) {
  return __builtin_bit_cast(u2, __builtin_amdgcn_ds_read_tr16_b64_v4i16((__attribute__((address_space(3))) s4*)p));
}
DEV u4 lds_r128(ldsp p) { return *(__attribute__((address_space(3))) u4*)p; }
DEV void lds_w128(ldsp p, u4 v) { *(__attribute__((address_space(3))) u4*)p = v; }
DEV void lds_w32(ldsp p, u32 v) { *(__attribute__((address_space(3))) u32*)p = v; }
DEV f4 lds_rf4(ldsp p) { return *(__attribute__((address_space(3))) f4*)p; }
DEV void lds_wf4(ldsp p, f4 v) { *(__attribute__((address_space(3))) f4*)p = v; }
DEV void glds16(const void* g, u32 ldsaddr) {
  u32 keep;
  asm volatile("s_mov_b32 %0, m0\n\ts_mov_b32 m0, %2\n\ts_nop 0\n\tglobal_load_lds_dwordx4 %1, off\n\ts_mov_b32 m0, %0"
               : "=&s"(keep) : "v"(g), "s"(ldsaddr) : "memory");
}
DEV void glds16_sc1(const void* g, u32 ldsaddr) {
  u32 keep;
  asm volatile("s_mov_b32 %0, m0\n\ts_mov_b32 m0, %2\n\ts_nop 0\n\tglobal_load_lds_dwordx4 %1, off sc1\n\ts_mov_b32 m0, %0"
               : "=&s"(keep) : "v"(g), "s"(ldsaddr) : "memory");
}
DEV void wait_vm0() { asm volatile("s_waitcnt vmcnt(0)" ::: "memory"); }
DEV void wait_lgkm0() { asm volatile("s_waitcnt lgkmcnt(0)" ::: "memory"); }
DEV void bar() { asm volatile("s_waitcnt lgkmcnt(0)\n\ts_barrier" ::: "memory"); }
DEV u32 rfl(u32 x) { return (u32)__builtin_amdgcn_readfirstlane((int)x); }
typedef __amdgpu_buffer_rsrc_t rsrc_t;
DEV rsrc_t mkrsrc(const void* p, u32 bytes) { return __builtin_amdgcn_make_buffer_rsrc((void*)p, (short)0, (int)bytes, 0x00020000); }
constexpr int AUX_SC1 = 16;
DEV u4 bload128(rsrc_t r, u32 off) { return __builtin_amdgcn_raw_buffer_load_b128(r, off, 0, AUX_SC1); }
DEV void refresh() { asm volatile("" ::: "memory"); }
DEV u4 bpoll128(rsrc_t r, u32 off) { return __builtin_amdgcn_raw_buffer_load_b128(r, off, 0, AUX_SC1); }
DEV u2 bload64(rsrc_t r, u32 off) { return __builtin_amdgcn_raw_buffer_load_b64(r, off, 0, AUX_SC1); }
DEV void bstore128(rsrc_t r, u32 off, u4 v) { __builtin_amdgcn_raw_buffer_store_b128(v, r, off, 0, AUX_SC1); }
DEV void bstore64(rsrc_t r, u32 off, u2 v) { __builtin_amdgcn_raw_buffer_store_b64(v, r, off, 0, AUX_SC1); }
DEV void bstore32(rsrc_t r, u32 off, u32 v) { __builtin_amdgcn_raw_buffer_store_b32(v, r, off, 0, AUX_SC1); }
DEV float swap32(float x) {
  auto r = __builtin_amdgcn_permlane32_swap(asu(x), asu(x), false, false);
  int l = __lane_id();
  return asf(l < 32 ? r[1] : r[0]);
}
DEV float red4_max(float x) {
  auto a = __builtin_amdgcn_permlane32_swap(asu(x), asu(x), false, false);
  x = fmaxf(asf(a[0]), asf(a[1]));
  auto b = __builtin_amdgcn_permlane16_swap(asu(x), asu(x), false, false);
  return fmaxf(asf(b[0]), asf(b[1]));
}
DEV float red4_sum(float x) {
  auto a = __builtin_amdgcn_permlane32_swap(asu(x), asu(x), false, false);
  x = asf(a[0]) + asf(a[1]);
  auto b = __builtin_amdgcn_permlane16_swap(asu(x), asu(x), false, false);
  return asf(b[0]) + asf(b[1]);
}
template <int CTRL>
DEV float dpp_add(float x) { return x + asf((u32)__builtin_amdgcn_update_dpp(0, (int)asu(x), CTRL, 0xf, 0xf, false)); }
DEV float wave_sum(float x) {
  x = dpp_add<0xB1>(x);
  x = dpp_add<0x4E>(x);
  x = dpp_add<0x141>(x);
  x = dpp_add<0x140>(x);
  return red4_sum(x);
}

DEV int swz256(int r) { return (r & 1) | ((((r >> 1) ^ (r >> 2)) & 1) << 1) | (((r >> 3) & 1) << 2); }
template <int NROWS>
DEV void stage_block(const __hip_bfloat16* src, int ld, u32 ldsoff, int w, int l) {
  constexpr int PER = NROWS / 16;
#pragma unroll
  for (int i = 0; i < PER; ++i) {
    int row = (w * PER + i) * 4 + (l >> 4);
    int ch = (l & 15) ^ (swz256(row) << 1);
    glds16((const char*)src + (size_t)row * ld * 2 + ch * 16, ldsoff + (u32)(w * PER + i) * 1024u);
  }
}
DEV u32 blk_addr(int row, int col) { return (u32)(row * 256 + (((col >> 4) ^ swz256(row)) << 5) + (col & 15) * 2); }
DEV u4 dense_frag(ldsp blk, int k0, int cb, int l) {
  int q = (l >> 2) & 3, g = l >> 4;
  u2 a = trread(blk + blk_addr(k0 + 8 * g + q, cb));
  u2 b = trread(blk + blk_addr(k0 + 8 * g + 4 + q, cb));
  return (u4){a.x, a.y, b.x, b.y};
}
DEV u8 sparse_frag(ldsp blk, int k0, int cb, int l) {
  int q = (l >> 2) & 3, g = l >> 4;
  int b = k0 + 16 * (g >> 1) + 4 * (g & 1) + (q & 1) + 8 * (q >> 1);
  u2 r0 = trread(blk + blk_addr(b, cb));
  u2 r1 = trread(blk + blk_addr(b + 2, cb));
  u2 r2 = trread(blk + blk_addr(b + 32, cb));
  u2 r3 = trread(blk + blk_addr(b + 34, cb));
  return (u8){r0.x, r0.y, r1.x, r1.y, r2.x, r2.y, r3.x, r3.y};
}

#define SSW 24
constexpr int S_SW = SSW;
DEV int nred(int s) { return (s >= S_SW && (s & 1)) ? 32 : 8; }
constexpr u32 FL_BASE = 0x320000;
constexpr u32 F_MOD = 0;
constexpr u32 F_PAR = 512;
constexpr u32 F_H = 1024;
constexpr u32 F_KV = 4096;
constexpr u32 F_P2 = 20480;
constexpr u32 F_H2 = F_P2 + 64 * 4096;
constexpr u32 F_ACT = F_H2 + 64 * 22 * 128;
constexpr u32 F_MODR = F_ACT + 64 * 16 * 128;
constexpr u32 F_HR = F_MODR + 8 * 128;
constexpr u32 FL_SIZE = F_HR + 8 * 128;
#define SLP 2
#define POLLMODE 2
#define PMARG 700
#define SEGT 20
constexpr int SPIN_FLAG = 30000;
DEV u32 ld_flag(const char* p) {
  u32 v;
  asm volatile("global_load_dword %0, %1, off sc1\n\ts_waitcnt vmcnt(0)" : "=v"(v) : "v"(p) : "memory");
  return v;
}
DEV void st_flag(const char* p, u32 v) { asm volatile("global_store_dword %0, %1, off sc1" ::"v"(p), "v"(v) : "memory"); }
DEV u4 ld_flag4(const char* p) {
  u4 v;
  asm volatile("global_load_dwordx4 %0, %1, off sc1\n\ts_waitcnt vmcnt(0)" : "=v"(v) : "v"(p) : "memory");
  return v;
}
struct Pred { u64 tprev; u32 gap; };
DEV void pred_init(Pred& q) { q.tprev = __builtin_amdgcn_s_memtime(); q.gap = 0; }
DEV bool poll_once(const char* p, int n, int l);
DEV void wait_words(const char* p, int n, int l);
DEV void wait_pred(const char* p, int n, int l, Pred& q) {
  wait_words(p, n, l);
  return;
  int fails = 0;
  if (!poll_once(p, n, l)) {
    while ((u32)(__builtin_amdgcn_s_memtime() - q.tprev) < q.gap) __builtin_amdgcn_s_sleep(SLP);
    for (; fails < SPIN_FLAG; ++fails) {
      if (poll_once(p, n, l)) break;
      __builtin_amdgcn_s_sleep(SLP);
    }
  }
  const u64 now = __builtin_amdgcn_s_memtime();
  const u32 per = min((u32)(now - q.tprev), 40000u), mg = fails ? PMARG : 2 * PMARG + 600;
  q.gap = per > mg ? per - mg : 0;
  q.tprev = now;
}
DEV bool poll_once(const char* p, int n, int l) {
  const int nq = (n + 3) >> 2;
  const char* q = (n == 1) ? p : p + l * 16;
  bool miss = false;
  if (l < nq) {
    if (n == 1) miss = ld_flag(q) == 0xffffffffu;
    else {
      u4 v = ld_flag4(q);
      miss = v.x == 0xffffffffu || (4 * l + 1 < n && v.y == 0xffffffffu) || (4 * l + 2 < n && v.z == 0xffffffffu) || (4 * l + 3 < n && v.w == 0xffffffffu);
    }
  }
  return __builtin_amdgcn_ballot_w64(miss) == 0;
}
DEV void wait_words(const char* p, int n, int l) {
  const int nq = (n + 3) >> 2;
  const char* q = (n == 1) ? p : p + l * 16;
  for (int it = 0; it < SPIN_FLAG; ++it) {
    bool miss = false;
    if (l < nq) {
      if (n == 1) miss = ld_flag(q) == 0xffffffffu;
      else {
        u4 v = ld_flag4(q);
        miss = v.x == 0xffffffffu || (4 * l + 1 < n && v.y == 0xffffffffu) || (4 * l + 2 < n && v.z == 0xffffffffu) || (4 * l + 3 < n && v.w == 0xffffffffu);
      }
    }
    if (__builtin_amdgcn_ballot_w64(miss) == 0) break;
    __builtin_amdgcn_s_sleep(SLP);
  }
}

struct Params {
  const __hip_bfloat16 *x, *mem0, *Wq, *Wk, *Wv, *Wo, *wg, *wu, *wd, *c, *wmod;
  __hip_bfloat16* out;
  char* ws;
};
DEV char* chain_base(const Params& P) { return (char*)((((size_t)(P.ws + OFF_CH)) + 0x1fffff) & ~(size_t)0x1fffff); }
DEV const char* flag_base(const Params& P) { return chain_base(P) + FL_BASE; }
DEV void tl(const Params& P, int i, bool core = false) {}

DEV void role_mod(const Params& P, int job, char* smem) {
  const int t = threadIdx.x;
  const int cc = t & 3, kg = t >> 2;
  float acc[8] = {0, 0, 0, 0, 0, 0, 0, 0};
  u4 wv[8];
#pragma unroll
  for (int i = 0; i < 8; ++i) wv[i] = *(const u4*)(P.wmod + (size_t)(kg * 8 + i) * 3072 + job * 32 + cc * 8);
  u4 cv = *(const u4*)(P.c + kg * 8);
#pragma unroll
  for (int i = 0; i < 8; ++i) {
    u32 cw = cv[i >> 1];
    float c = (i & 1) ? bfhi(cw) : bflo(cw);
    float sc = bfr(c * __builtin_amdgcn_rcpf(1.f + __expf(-c)));
#pragma unroll
    for (int j = 0; j < 4; ++j) { acc[2 * j] += sc * bflo(wv[i][j]); acc[2 * j + 1] += sc * bfhi(wv[i][j]); }
  }
#pragma unroll
  for (int j = 0; j < 8; ++j) acc[j] = red4_sum(dpp_add<0x128>(dpp_add<0x124>(acc[j])));
  float* red = (float*)smem;
  if ((t & 63) < 4)
#pragma unroll
    for (int j = 0; j < 8; ++j) red[(t >> 6) * 32 + cc * 8 + j] = acc[j];
  __syncthreads();
  if (t < 32) {
    float v = bfr(red[t] + red[32 + t] + red[64 + t] + red[96 + t]);
    rsrc_t r = mkrsrc(P.ws + OFF_MOD, 3072 * 4);
    bstore32(r, (job * 32 + t) * 4, asu(v));
    wait_vm0();
    if (t == 0) st_flag(flag_base(P) + F_MOD + job * 4, 1u);
    if (job < 32 && t >= 8 && t < 16) st_flag(flag_base(P) + F_MODR + (t - 8) * 128 + job * 4, 1u);
  }
  __syncthreads();
}

DEV void ln1_stats(u4 xv, float (&v)[8]) {
#pragma unroll
  for (int j = 0; j < 4; ++j) { v[2 * j] = bflo(xv[j]); v[2 * j + 1] = bfhi(xv[j]); }
  float s = 0;
#pragma unroll
  for (int j = 0; j < 8; ++j) s += v[j];
  float mean = wave_sum(s) * (1.f / DM);
  float q = 0;
#pragma unroll
  for (int j = 0; j < 8; ++j) { v[j] -= mean; q += v[j] * v[j]; }
  float rstd = rsqrtf(wave_sum(q) * (1.f / DM) + 1e-5f);
#pragma unroll
  for (int j = 0; j < 8; ++j) v[j] *= rstd;
}
#define PREF 2
#define NCAL 5
#define CALPOS 3
DEV void role_calib(const Params& P, char* red, bool publish, int inv) {
  const int t = threadIdx.x, w = rfl(t >> 6);
  char* base = chain_base(P) + 0x400000 + w * 0x20000 + (publish ? 0 : 0x80000);
  u32 best[2] = {0xffffffffu, 0xffffffffu};
#pragma unroll 1
  for (int k = 0; k < NCAL; ++k) {
#pragma unroll
    for (int par = 0; par < 2; ++par) {
      const char* p = base + par * 8192 + k * 16384 + (t & 63) * 4;
      u64 t0, t1;
      u32 v;
      asm volatile("s_memtime %0\n\ts_waitcnt lgkmcnt(0)\n\tglobal_store_dword %3, %4, off sc1\n\ts_waitcnt vmcnt(0)\n\t"
                   "global_load_dword %2, %3, off sc1\n\ts_waitcnt vmcnt(0)\n\ts_memtime %1\n\ts_waitcnt lgkmcnt(0)"
                   : "=&s"(t0), "=&s"(t1), "=&v"(v) : "v"(p), "v"(k) : "memory");
      best[par] = min(best[par], (u32)(t1 - t0));
    }
  }
  u32* r = (u32*)red;
  if ((t & 63) == 0) { r[w * 2] = best[0]; r[w * 2 + 1] = best[1]; }
  __syncthreads();
  if (t == 0) {
    u32 b0 = min(min(r[0], r[2]), min(r[4], r[6])), b1 = min(min(r[1], r[3]), min(r[5], r[7]));
    u32 par = (b1 < b0 ? 1u : 0u) ^ inv;
    if (publish) st_flag(flag_base(P) + F_PAR, par);
  }
  __syncthreads();
}

DEV void role_ln1(const Params& P, int j, char* smem) {
  const int t = threadIdx.x, l = t & 63, w = rfl(t >> 6);
  const char* fl = flag_base(P);
  u4 xv[8];
#pragma unroll
  for (int p = 0; p < 8; ++p) xv[p] = *(const u4*)(P.x + (size_t)(p * 256 + j * 4 + w) * DM + l * 8);
  float v0[8];
  ln1_stats(xv[0], v0);
  u32 pf[16];
#pragma unroll
  for (int i = 0; i < 16; ++i) pf[i] = 0;
  if (j >= 32) {
    const int h = j & 31;
    const char* a = (const char*)P.Wq + h * 256 + (t >> 1) * 8192 + (t & 1) * 128;
    const char* b = (const char*)P.Wk + h * 256 + (t >> 1) * 8192 + (t & 1) * 128;
    const char* c = (const char*)P.Wv + h * 256 + (t >> 1) * 8192 + (t & 1) * 128;
    const char* d = (const char*)P.Wo + h * 131072 + t * 128;
#pragma unroll
    for (int i = 0; i < 4; ++i) pf[i] = *(const u32*)(a + i * 128 * 8192);
#pragma unroll
    for (int i = 0; i < 4; ++i) pf[4 + i] = *(const u32*)(b + i * 128 * 8192);
#pragma unroll
    for (int i = 0; i < 4; ++i) pf[8 + i] = *(const u32*)(c + i * 128 * 8192);
#pragma unroll
    for (int i = 0; i < 4; ++i) pf[12 + i] = *(const u32*)(d + i * 32768);
  }
  tl(P, 4);
  if (w == 0) wait_words(fl + F_MODR + (j & 7) * 128, 32, l);
  bar();
#pragma unroll
  for (int i = 0; i < (PREF == 2 ? 16 : 8); ++i) asm volatile("" ::"v"(pf[i]));
  tl(P, 5);
  rsrc_t rm = mkrsrc(P.ws + OFF_MOD, 3072 * 4);
  u4 sa0 = bload128(rm, l * 32), sa1 = bload128(rm, l * 32 + 16);
  u4 ca0 = bload128(rm, 2048 + l * 32), ca1 = bload128(rm, 2048 + l * 32 + 16);
  float sa[8], ca[8];
#pragma unroll
  for (int i = 0; i < 8; ++i) {
    sa[i] = asf(i < 4 ? sa0[i & 3] : sa1[i & 3]);
    ca[i] = bfr(1.f + asf(i < 4 ? ca0[i & 3] : ca1[i & 3]));
  }
  rsrc_t rh = mkrsrc(P.ws + OFF_H, S_ * DM * 2);
#pragma unroll
  for (int p = 0; p < 8; ++p) {
    float v[8];
    if (p == 0) {
#pragma unroll
      for (int i = 0; i < 8; ++i) v[i] = v0[i];
    } else ln1_stats(xv[p], v);
    float h[8];
#pragma unroll
    for (int i = 0; i < 8; ++i) h[i] = v[i] * ca[i] + sa[i];
    bstore128(rh, ((p * 256 + j * 4 + w) * DM + l * 8) * 2,
              (u4){cvtpk(h[0], h[1]), cvtpk(h[2], h[3]), cvtpk(h[4], h[5]), cvtpk(h[6], h[7])});
    if (p == 0 || p == 7) {
      wait_vm0();
      bar();
      if (p == 0) {
        if (t == 0) st_flag(fl + F_H + j * 4, 1u);
        if (j < 32 && t >= 8 && t < 16) st_flag(fl + F_HR + (t - 8) * 128 + ((j >> 3) & 1) * 64 + (j >> 4) * 32 + (j & 7) * 4, 1u);
        tl(P, 6);
      }
      else if (t >= 1 && t < 8) st_flag(fl + F_H + (t * 64 + j) * 4, 1u);
    }
  }
}

constexpr u32 HROW = 1040, HBUF = 32 * HROW;
#define APHASE 0
#define RPHASE 0
#define PACEA 0
#define PACEK 0
template <bool PERM, int PACE = 0>
DEV void stage_rows(const char* base  , u32 ldsoff, int w, int l) {
#pragma unroll
  for (int i = 0; i < 8; ++i) {
    const int f = w * 8 + i, tt = f >> 4, r = f & 15;
    const int tok = PERM ? (8 * (r >> 2) + 4 * tt + (r & 3)) : f;
    glds16_sc1(base + tok * 1024 + l * 16, ldsoff + (u32)f * HROW);
    if (PACE) __builtin_amdgcn_s_sleep(PACE);
  }
}

DEV void role_kv(const Params& P, int h, int s0, int ds, ldsp lds, char* smem) {
  const int t = threadIdx.x, l = t & 63, w = rfl(t >> 6);
  const int g = l >> 4;
  u4 W[4][16];
  const __hip_bfloat16* Wk = P.Wk + h * DH;
  const __hip_bfloat16* Wv = P.Wv + h * DH;
  stage_block<128>(Wk, NH * DH, 0, w, l);
  stage_block<128>(Wv, NH * DH, 32768, w, l);
#pragma unroll
  for (int u = 0; u < 4; ++u) {
    if (u < 3) {
      stage_block<128>(Wk + (size_t)(u + 1) * 128 * NH * DH, NH * DH, ((u + 1) & 1) * 65536, w, l);
      stage_block<128>(Wv + (size_t)(u + 1) * 128 * NH * DH, NH * DH, ((u + 1) & 1) * 65536 + 32768, w, l);
      asm volatile("s_waitcnt vmcnt(16)" ::: "memory");
    } else wait_vm0();
    bar();
    ldsp blk = lds + (u & 1) * 65536 + (w >> 1) * 32768;
#pragma unroll
    for (int pt = 0; pt < 4; ++pt)
#pragma unroll
      for (int k4 = 0; k4 < 4; ++k4) {
        int p = (w & 1) * 2 + (pt >> 1), tt = pt & 1;
        int cb = (w < 2) ? 32 * p + 16 * tt + 4 * (l & 3) : 32 * p + 8 * (l & 3) + 4 * tt;
        W[pt][u * 4 + k4] = dense_frag(blk, k4 * 32, cb, l);
      }
    bar();
  }
  const char* H = P.ws + OFF_H;
  rsrc_t rkv = mkrsrc(P.ws + OFF_KV, 0x2000000);
  const char* fl = flag_base(P);
  int buf = 0, hpass = -1;
  tl(P, 4);
  if (w == 0) wait_words(fl + F_HR + (h & 7) * 128 + s0 * 64, 16, l);
  bar();
  tl(P, 5);
  stage_rows<true>(H + (size_t)s0 * 32768, 0, w, l);
  for (int s = s0; s < NSEG; s += ds) {
    wait_vm0();
    bar();
    if (s > s0 && t == 0) st_flag(fl + F_KV + h * 512 + (s - ds) * 4, 1u);
    if (s > s0) tl(P, 8 + s - ds); else tl(P, 2);
    const bool newpass = s > s0 && s + ds < NSEG && ((s + ds) >> 3) > hpass;
    if (!newpass && s + ds < NSEG) stage_rows<true, PACEK>(H + (size_t)(s + ds) * 32768, (buf ^ 1) * HBUF, w, l);
    ldsp hb = lds + buf * HBUF + (l & 15) * HROW + g * 16;
    f4 acc[4][2];
#pragma unroll
    for (int pt = 0; pt < 4; ++pt) { acc[pt][0] = (f4){0, 0, 0, 0}; acc[pt][1] = (f4){0, 0, 0, 0}; }
    if (w < 2) {
#pragma unroll
      for (int kk = 0; kk < 16; ++kk) {
        u4 h0 = lds_r128(hb + kk * 64), h1 = lds_r128(hb + 16 * HROW + kk * 64);
#pragma unroll
        for (int pt = 0; pt < 4; ++pt) { acc[pt][0] = mfma32(W[pt][kk], h0, acc[pt][0]); acc[pt][1] = mfma32(W[pt][kk], h1, acc[pt][1]); }
      }
#pragma unroll
      for (int pp = 0; pp < 2; ++pp)
#pragma unroll
        for (int tt = 0; tt < 2; ++tt) {
          f4 a = acc[2 * pp][tt], b = acc[2 * pp + 1][tt];
          u4 o = {cvtpk(a[0], b[0]), cvtpk(a[1], b[1]), cvtpk(a[2], b[2]), cvtpk(a[3], b[3])};
          int p = w * 2 + pp;
          bstore128(rkv, (u32)(s * NH + h) * 16384u + (tt * 4 + p) * 1024 + l * 16, o);
        }
    } else {
#pragma unroll
      for (int kk = 0; kk < 16; ++kk) {
        u4 h0 = lds_r128(hb + kk * 64), h1 = lds_r128(hb + 16 * HROW + kk * 64);
#pragma unroll
        for (int pt = 0; pt < 4; ++pt) { acc[pt][0] = mfma32(h0, W[pt][kk], acc[pt][0]); acc[pt][1] = mfma32(h1, W[pt][kk], acc[pt][1]); }
      }
#pragma unroll
      for (int pt = 0; pt < 4; ++pt) {
        f4 a = acc[pt][0], b = acc[pt][1];
        u4 o = {cvtpk(a[0], a[1]), cvtpk(a[2], a[3]), cvtpk(b[0], b[1]), cvtpk(b[2], b[3])};
        int et = (w - 2) * 4 + pt;
        bstore128(rkv, (u32)(s * NH + h) * 16384u + 8192 + et * 1024 + l * 16, o);
      }
    }
    if (newpass) {
      hpass = (s + ds) >> 3;
      if (w == 0) wait_words(fl + F_H + hpass * 256, 64, l);
      bar();
      stage_rows<true>(H + (size_t)(s + ds) * 32768, (buf ^ 1) * HBUF, w, l);
    }
    buf ^= 1;
  }
  wait_vm0();
  bar();
  if (t == 0) st_flag(fl + F_KV + h * 512 + (NSEG - ds + s0) * 4, 1u);
  tl(P, 8 + NSEG - ds + s0);
  (void)g;
}

constexpr u32 CL_M = 131072, CL_KV = 139264, CL_QK = 155648, CL_O = 160256, CL_QROW = 288;
DEV int mrow(int r) { return 2 * (r >> 2) + (r & 1); }
constexpr u32 SENT = 0xffffffffu;
constexpr int SPIN_MAX = 200000;
typedef __attribute__((ext_vector_type(16))) u32 u16v;
DEV void role_chain(const Params& P, int h, char* chbase) {
  const int t = threadIdx.x, l = t & 63, w = rfl(t >> 6);
  const int rho = l & 15, g = l >> 4;
  u16v c0, c2;
  u4 c1;
  const int hf = (rho >> 1) & 1, mr = mrow(rho), r8 = rho & 7;
  c0[0] = hf ? 0xEEEEu : 0x4444u;
  c0[1] = l * 16 + CL_M;
  c0[2] = l * 16 + w * 16384;
  c0[3] = l * 16 + w * 16384 + 65536;
  c0[4] = CL_QK + 2 * g * CL_QROW + w * 64 + rho * 4;
  c0[5] = CL_QK + mr * CL_QROW + g * 16;
  c0[6] = CL_O + (w >> 1) * 1024 + ((2 * (w & 1) + (g >> 1)) * 16 + 4 * (rho >> 2) + 2 * (g & 1) + (rho & 1)) * 16;
  c0[7] = l * 16;
  c0[8] = (g & 1) * 16384 + rho * 16 + (g >> 1) * 8;
  c0[9] = (4 * (r8 >> 1) + 2 * (g >> 1) + (r8 & 1)) * 16 + (g & 1) * 8;
  c0[10] = (g * 8 + mr) * 16;
  c0[11] = l * 8;
#pragma unroll
  for (int j = 0; j < 4; ++j) c0[12 + j] = ((rho & 7) == 2 * j ? 0x3f80u : 0u) | ((rho & 7) == 2 * j + 1 ? 0x3f800000u : 0u);
  c1[0] = 0x3f803f80u;
  c1[1] = 0u;
  c1[2] = c1[3] = 0x3f803f80u;
  const u2 c1b = {0x3f803f80u, 0x3f803f80u};
  const int q = (l >> 2) & 3;
  const int b = 16 * (g >> 1) + 4 * (g & 1) + (q & 1) + 8 * (q >> 1);
#pragma unroll
  for (int i = 0; i < 4; ++i) {
    const int r = 4 * i + g;
    const int ch = rho ^ (swz256(r) << 1);
    c2[i] = (64 * w + r) * 8192 + ch * 16;
    c2[4 + i] = (16 * w + r) * 1024 + ch * 16;
  }
#pragma unroll
  for (int tt = 0; tt < 2; ++tt)
#pragma unroll
    for (int r = 0; r < 2; ++r) c2[8 + 2 * tt + r] = blk_addr(b + 2 * r, 32 * w + 16 * tt + 4 * (l & 3));
#pragma unroll
  for (int r = 0; r < 2; ++r) {
    c2[12 + r] = blk_addr(b + 2 * r, 32 * w + 8 * (l & 3));
    c2[14 + r] = w * 16384 + blk_addr(b + 2 * r, 4 * (l & 3));
  }
  u32 m0off = mr * 1024 + (16 * g + 8 * hf) * 2;
  const char* wq = (const char*)(P.Wq + h * DH);
  const char* wk = (const char*)(P.Wk + h * DH);
  const char* wv = (const char*)(P.Wv + h * DH);
  const char* wo = (const char*)(P.Wo + (size_t)h * DH * DM);
  const char* kv = P.ws + OFF_KV + (size_t)h * 16384;
  const char* kvm = P.ws + OFF_KVM + (size_t)h * 8192;
  const char* ch = chbase;
  const char* stp = P.ws + (200u << 20) + (size_t)h * 16384;
  asm volatile(
  ".p2align 7\n"
  "s_mov_b64 s[60:61], s[48:49]\n"
  "s_mov_b32 s54, 0xfc2000\n"
  "s_mov_b32 s55, 0x20000\n"
  "s_mov_b32 s58, 0x3fe000\n"
  "s_mov_b32 s59, 0x20000\n"
  "s_mov_b32 s70, 0x33333333\n"
  "s_mov_b32 s71, 0x33333333\n"
  "s_mov_b32 s92, 0x00ff00ff\n"
  "s_mov_b32 s93, 0x00ff00ff\n"
  "s_mov_b32 s72, 0x3e0293ee\n"
  "s_lshl_b32 s78, s68, 14\n"
  "s_lshl_b32 s75, s68, 12\n"
  "v_mov_b32 v24, 0\n"
  "v_mov_b32 v25, 0\n"
  "v_mov_b32 v26, 0\n"
  "v_mov_b32 v27, 0\n"
  "v_mov_b32 v28, 0\n"
  "v_mov_b32 v29, 0\n"
  "v_mov_b32 v30, 0\n"
  "v_mov_b32 v31, 0\n"
  "s_add_u32 s76, s40, 0\n"
  "s_addc_u32 s77, s41, 0\n"
  "s_add_u32 m0, s78, 0\n"
  "s_nop 0\n"
  "global_load_lds_dwordx4 v96, s[76:77]\n"
  "s_add_u32 m0, s78, 1024\n"
  "s_nop 0\n"
  "global_load_lds_dwordx4 v97, s[76:77]\n"
  "s_add_u32 m0, s78, 2048\n"
  "s_nop 0\n"
  "global_load_lds_dwordx4 v98, s[76:77]\n"
  "s_add_u32 m0, s78, 3072\n"
  "s_nop 0\n"
  "global_load_lds_dwordx4 v99, s[76:77]\n"
  "s_add_u32 s76, s40, 131072\n"
  "s_addc_u32 s77, s41, 0\n"
  "s_add_u32 m0, s78, 4096\n"
  "s_nop 0\n"
  "global_load_lds_dwordx4 v96, s[76:77]\n"
  "s_add_u32 m0, s78, 5120\n"
  "s_nop 0\n"
  "global_load_lds_dwordx4 v97, s[76:77]\n"
  "s_add_u32 m0, s78, 6144\n"
  "s_nop 0\n"
  "global_load_lds_dwordx4 v98, s[76:77]\n"
  "s_add_u32 m0, s78, 7168\n"
  "s_nop 0\n"
  "global_load_lds_dwordx4 v99, s[76:77]\n"
  "s_add_u32 s76, s40, 262144\n"
  "s_addc_u32 s77, s41, 0\n"
  "s_add_u32 m0, s78, 8192\n"
  "s_nop 0\n"
  "global_load_lds_dwordx4 v96, s[76:77]\n"
  "s_add_u32 m0, s78, 9216\n"
  "s_nop 0\n"
  "global_load_lds_dwordx4 v97, s[76:77]\n"
  "s_add_u32 m0, s78, 10240\n"
  "s_nop 0\n"
  "global_load_lds_dwordx4 v98, s[76:77]\n"
  "s_add_u32 m0, s78, 11264\n"
  "s_nop 0\n"
  "global_load_lds_dwordx4 v99, s[76:77]\n"
  "s_add_u32 s76, s40, 393216\n"
  "s_addc_u32 s77, s41, 0\n"
  "s_add_u32 m0, s78, 12288\n"
  "s_nop 0\n"
  "global_load_lds_dwordx4 v96, s[76:77]\n"
  "s_add_u32 m0, s78, 13312\n"
  "s_nop 0\n"
  "global_load_lds_dwordx4 v97, s[76:77]\n"
  "s_add_u32 m0, s78, 14336\n"
  "s_nop 0\n"
  "global_load_lds_dwordx4 v98, s[76:77]\n"
  "s_add_u32 m0, s78, 15360\n"
  "s_nop 0\n"
  "global_load_lds_dwordx4 v99, s[76:77]\n"
  "s_add_u32 s76, s40, 2097152\n"
  "s_addc_u32 s77, s41, 0\n"
  "s_add_u32 m0, s78, 65536\n"
  "s_nop 0\n"
  "global_load_lds_dwordx4 v96, s[76:77]\n"
  "s_add_u32 m0, s78, 66560\n"
  "s_nop 0\n"
  "global_load_lds_dwordx4 v97, s[76:77]\n"
  "s_add_u32 m0, s78, 67584\n"
  "s_nop 0\n"
  "global_load_lds_dwordx4 v98, s[76:77]\n"
  "s_add_u32 m0, s78, 68608\n"
  "s_nop 0\n"
  "global_load_lds_dwordx4 v99, s[76:77]\n"
  "s_add_u32 s76, s40, 2228224\n"
  "s_addc_u32 s77, s41, 0\n"
  "s_add_u32 m0, s78, 69632\n"
  "s_nop 0\n"
  "global_load_lds_dwordx4 v96, s[76:77]\n"
  "s_add_u32 m0, s78, 70656\n"
  "s_nop 0\n"
  "global_load_lds_dwordx4 v97, s[76:77]\n"
  "s_add_u32 m0, s78, 71680\n"
  "s_nop 0\n"
  "global_load_lds_dwordx4 v98, s[76:77]\n"
  "s_add_u32 m0, s78, 72704\n"
  "s_nop 0\n"
  "global_load_lds_dwordx4 v99, s[76:77]\n"
  "s_add_u32 s76, s40, 2359296\n"
  "s_addc_u32 s77, s41, 0\n"
  "s_add_u32 m0, s78, 73728\n"
  "s_nop 0\n"
  "global_load_lds_dwordx4 v96, s[76:77]\n"
  "s_add_u32 m0, s78, 74752\n"
  "s_nop 0\n"
  "global_load_lds_dwordx4 v97, s[76:77]\n"
  "s_add_u32 m0, s78, 75776\n"
  "s_nop 0\n"
  "global_load_lds_dwordx4 v98, s[76:77]\n"
  "s_add_u32 m0, s78, 76800\n"
  "s_nop 0\n"
  "global_load_lds_dwordx4 v99, s[76:77]\n"
  "s_add_u32 s76, s40, 2490368\n"
  "s_addc_u32 s77, s41, 0\n"
  "s_add_u32 m0, s78, 77824\n"
  "s_nop 0\n"
  "global_load_lds_dwordx4 v96, s[76:77]\n"
  "s_add_u32 m0, s78, 78848\n"
  "s_nop 0\n"
  "global_load_lds_dwordx4 v97, s[76:77]\n"
  "s_add_u32 m0, s78, 79872\n"
  "s_nop 0\n"
  "global_load_lds_dwordx4 v98, s[76:77]\n"
  "s_add_u32 m0, s78, 80896\n"
  "s_nop 0\n"
  "global_load_lds_dwordx4 v99, s[76:77]\n"
  "s_waitcnt vmcnt(16)\n"
  "s_barrier\n"
  "ds_read_b64_tr_b16 a[0:1], v104\n"
  "ds_read_b64_tr_b16 a[2:3], v105\n"
  "ds_read_b64_tr_b16 a[4:5], v104 offset:8192\n"
  "ds_read_b64_tr_b16 a[6:7], v105 offset:8192\n"
  "ds_read_b64_tr_b16 a[8:9], v106\n"
  "ds_read_b64_tr_b16 a[10:11], v107\n"
  "ds_read_b64_tr_b16 a[12:13], v106 offset:8192\n"
  "ds_read_b64_tr_b16 a[14:15], v107 offset:8192\n"
  "ds_read_b64_tr_b16 a[32:33], v104 offset:16384\n"
  "ds_read_b64_tr_b16 a[34:35], v105 offset:16384\n"
  "ds_read_b64_tr_b16 a[36:37], v104 offset:24576\n"
  "ds_read_b64_tr_b16 a[38:39], v105 offset:24576\n"
  "ds_read_b64_tr_b16 a[40:41], v106 offset:16384\n"
  "ds_read_b64_tr_b16 a[42:43], v107 offset:16384\n"
  "ds_read_b64_tr_b16 a[44:45], v106 offset:24576\n"
  "ds_read_b64_tr_b16 a[46:47], v107 offset:24576\n"
  "ds_read_b64_tr_b16 a[64:65], v104 offset:32768\n"
  "ds_read_b64_tr_b16 a[66:67], v105 offset:32768\n"
  "ds_read_b64_tr_b16 a[68:69], v104 offset:40960\n"
  "ds_read_b64_tr_b16 a[70:71], v105 offset:40960\n"
  "ds_read_b64_tr_b16 a[72:73], v106 offset:32768\n"
  "ds_read_b64_tr_b16 a[74:75], v107 offset:32768\n"
  "ds_read_b64_tr_b16 a[76:77], v106 offset:40960\n"
  "ds_read_b64_tr_b16 a[78:79], v107 offset:40960\n"
  "ds_read_b64_tr_b16 a[96:97], v104 offset:49152\n"
  "ds_read_b64_tr_b16 a[98:99], v105 offset:49152\n"
  "ds_read_b64_tr_b16 a[100:101], v104 offset:57344\n"
  "ds_read_b64_tr_b16 a[102:103], v105 offset:57344\n"
  "ds_read_b64_tr_b16 a[104:105], v106 offset:49152\n"
  "ds_read_b64_tr_b16 a[106:107], v107 offset:49152\n"
  "ds_read_b64_tr_b16 a[108:109], v106 offset:57344\n"
  "ds_read_b64_tr_b16 a[110:111], v107 offset:57344\n"
  "v_xor_b32 v104, 0x10000, v104\n"
  "v_xor_b32 v105, 0x10000, v105\n"
  "v_xor_b32 v106, 0x10000, v106\n"
  "v_xor_b32 v107, 0x10000, v107\n"
  "v_xor_b32 v108, 0x10000, v108\n"
  "v_xor_b32 v109, 0x10000, v109\n"
  "v_xor_b32 v110, 0x10000, v110\n"
  "v_xor_b32 v111, 0x10000, v111\n"
  "s_waitcnt lgkmcnt(0)\n"
  "s_barrier\n"
  "s_add_u32 s76, s42, 0\n"
  "s_addc_u32 s77, s43, 0\n"
  "s_add_u32 m0, s78, 0\n"
  "s_nop 0\n"
  "global_load_lds_dwordx4 v96, s[76:77]\n"
  "s_add_u32 m0, s78, 1024\n"
  "s_nop 0\n"
  "global_load_lds_dwordx4 v97, s[76:77]\n"
  "s_add_u32 m0, s78, 2048\n"
  "s_nop 0\n"
  "global_load_lds_dwordx4 v98, s[76:77]\n"
  "s_add_u32 m0, s78, 3072\n"
  "s_nop 0\n"
  "global_load_lds_dwordx4 v99, s[76:77]\n"
  "s_add_u32 s76, s42, 131072\n"
  "s_addc_u32 s77, s43, 0\n"
  "s_add_u32 m0, s78, 4096\n"
  "s_nop 0\n"
  "global_load_lds_dwordx4 v96, s[76:77]\n"
  "s_add_u32 m0, s78, 5120\n"
  "s_nop 0\n"
  "global_load_lds_dwordx4 v97, s[76:77]\n"
  "s_add_u32 m0, s78, 6144\n"
  "s_nop 0\n"
  "global_load_lds_dwordx4 v98, s[76:77]\n"
  "s_add_u32 m0, s78, 7168\n"
  "s_nop 0\n"
  "global_load_lds_dwordx4 v99, s[76:77]\n"
  "s_add_u32 s76, s42, 262144\n"
  "s_addc_u32 s77, s43, 0\n"
  "s_add_u32 m0, s78, 8192\n"
  "s_nop 0\n"
  "global_load_lds_dwordx4 v96, s[76:77]\n"
  "s_add_u32 m0, s78, 9216\n"
  "s_nop 0\n"
  "global_load_lds_dwordx4 v97, s[76:77]\n"
  "s_add_u32 m0, s78, 10240\n"
  "s_nop 0\n"
  "global_load_lds_dwordx4 v98, s[76:77]\n"
  "s_add_u32 m0, s78, 11264\n"
  "s_nop 0\n"
  "global_load_lds_dwordx4 v99, s[76:77]\n"
  "s_add_u32 s76, s42, 393216\n"
  "s_addc_u32 s77, s43, 0\n"
  "s_add_u32 m0, s78, 12288\n"
  "s_nop 0\n"
  "global_load_lds_dwordx4 v96, s[76:77]\n"
  "s_add_u32 m0, s78, 13312\n"
  "s_nop 0\n"
  "global_load_lds_dwordx4 v97, s[76:77]\n"
  "s_add_u32 m0, s78, 14336\n"
  "s_nop 0\n"
  "global_load_lds_dwordx4 v98, s[76:77]\n"
  "s_add_u32 m0, s78, 15360\n"
  "s_nop 0\n"
  "global_load_lds_dwordx4 v99, s[76:77]\n"
  "s_waitcnt vmcnt(16)\n"
  "s_barrier\n"
  "ds_read_b64_tr_b16 a[128:129], v104\n"
  "ds_read_b64_tr_b16 a[130:131], v105\n"
  "ds_read_b64_tr_b16 a[132:133], v104 offset:8192\n"
  "ds_read_b64_tr_b16 a[134:135], v105 offset:8192\n"
  "ds_read_b64_tr_b16 a[136:137], v106\n"
  "ds_read_b64_tr_b16 a[138:139], v107\n"
  "ds_read_b64_tr_b16 a[140:141], v106 offset:8192\n"
  "ds_read_b64_tr_b16 a[142:143], v107 offset:8192\n"
  "ds_read_b64_tr_b16 a[160:161], v104 offset:16384\n"
  "ds_read_b64_tr_b16 a[162:163], v105 offset:16384\n"
  "ds_read_b64_tr_b16 a[164:165], v104 offset:24576\n"
  "ds_read_b64_tr_b16 a[166:167], v105 offset:24576\n"
  "ds_read_b64_tr_b16 a[168:169], v106 offset:16384\n"
  "ds_read_b64_tr_b16 a[170:171], v107 offset:16384\n"
  "ds_read_b64_tr_b16 a[172:173], v106 offset:24576\n"
  "ds_read_b64_tr_b16 a[174:175], v107 offset:24576\n"
  "ds_read_b64_tr_b16 a[192:193], v104 offset:32768\n"
  "ds_read_b64_tr_b16 a[194:195], v105 offset:32768\n"
  "ds_read_b64_tr_b16 a[196:197], v104 offset:40960\n"
  "ds_read_b64_tr_b16 a[198:199], v105 offset:40960\n"
  "ds_read_b64_tr_b16 a[200:201], v106 offset:32768\n"
  "ds_read_b64_tr_b16 a[202:203], v107 offset:32768\n"
  "ds_read_b64_tr_b16 a[204:205], v106 offset:40960\n"
  "ds_read_b64_tr_b16 a[206:207], v107 offset:40960\n"
  "ds_read_b64_tr_b16 a[224:225], v104 offset:49152\n"
  "ds_read_b64_tr_b16 a[226:227], v105 offset:49152\n"
  "ds_read_b64_tr_b16 a[228:229], v104 offset:57344\n"
  "ds_read_b64_tr_b16 a[230:231], v105 offset:57344\n"
  "ds_read_b64_tr_b16 a[232:233], v106 offset:49152\n"
  "ds_read_b64_tr_b16 a[234:235], v107 offset:49152\n"
  "ds_read_b64_tr_b16 a[236:237], v106 offset:57344\n"
  "ds_read_b64_tr_b16 a[238:239], v107 offset:57344\n"
  "v_xor_b32 v104, 0x10000, v104\n"
  "v_xor_b32 v105, 0x10000, v105\n"
  "v_xor_b32 v106, 0x10000, v106\n"
  "v_xor_b32 v107, 0x10000, v107\n"
  "v_xor_b32 v108, 0x10000, v108\n"
  "v_xor_b32 v109, 0x10000, v109\n"
  "v_xor_b32 v110, 0x10000, v110\n"
  "v_xor_b32 v111, 0x10000, v111\n"
  "s_waitcnt lgkmcnt(0)\n"
  "s_barrier\n"
  "s_add_u32 s76, s42, 2097152\n"
  "s_addc_u32 s77, s43, 0\n"
  "s_add_u32 m0, s78, 65536\n"
  "s_nop 0\n"
  "global_load_lds_dwordx4 v96, s[76:77]\n"
  "s_add_u32 m0, s78, 66560\n"
  "s_nop 0\n"
  "global_load_lds_dwordx4 v97, s[76:77]\n"
  "s_add_u32 m0, s78, 67584\n"
  "s_nop 0\n"
  "global_load_lds_dwordx4 v98, s[76:77]\n"
  "s_add_u32 m0, s78, 68608\n"
  "s_nop 0\n"
  "global_load_lds_dwordx4 v99, s[76:77]\n"
  "s_add_u32 s76, s42, 2228224\n"
  "s_addc_u32 s77, s43, 0\n"
  "s_add_u32 m0, s78, 69632\n"
  "s_nop 0\n"
  "global_load_lds_dwordx4 v96, s[76:77]\n"
  "s_add_u32 m0, s78, 70656\n"
  "s_nop 0\n"
  "global_load_lds_dwordx4 v97, s[76:77]\n"
  "s_add_u32 m0, s78, 71680\n"
  "s_nop 0\n"
  "global_load_lds_dwordx4 v98, s[76:77]\n"
  "s_add_u32 m0, s78, 72704\n"
  "s_nop 0\n"
  "global_load_lds_dwordx4 v99, s[76:77]\n"
  "s_add_u32 s76, s42, 2359296\n"
  "s_addc_u32 s77, s43, 0\n"
  "s_add_u32 m0, s78, 73728\n"
  "s_nop 0\n"
  "global_load_lds_dwordx4 v96, s[76:77]\n"
  "s_add_u32 m0, s78, 74752\n"
  "s_nop 0\n"
  "global_load_lds_dwordx4 v97, s[76:77]\n"
  "s_add_u32 m0, s78, 75776\n"
  "s_nop 0\n"
  "global_load_lds_dwordx4 v98, s[76:77]\n"
  "s_add_u32 m0, s78, 76800\n"
  "s_nop 0\n"
  "global_load_lds_dwordx4 v99, s[76:77]\n"
  "s_add_u32 s76, s42, 2490368\n"
  "s_addc_u32 s77, s43, 0\n"
  "s_add_u32 m0, s78, 77824\n"
  "s_nop 0\n"
  "global_load_lds_dwordx4 v96, s[76:77]\n"
  "s_add_u32 m0, s78, 78848\n"
  "s_nop 0\n"
  "global_load_lds_dwordx4 v97, s[76:77]\n"
  "s_add_u32 m0, s78, 79872\n"
  "s_nop 0\n"
  "global_load_lds_dwordx4 v98, s[76:77]\n"
  "s_add_u32 m0, s78, 80896\n"
  "s_nop 0\n"
  "global_load_lds_dwordx4 v99, s[76:77]\n"
  "s_waitcnt vmcnt(16)\n"
  "s_barrier\n"
  "ds_read_b64_tr_b16 a[16:17], v104\n"
  "ds_read_b64_tr_b16 a[18:19], v105\n"
  "ds_read_b64_tr_b16 a[20:21], v104 offset:8192\n"
  "ds_read_b64_tr_b16 a[22:23], v105 offset:8192\n"
  "ds_read_b64_tr_b16 a[24:25], v106\n"
  "ds_read_b64_tr_b16 a[26:27], v107\n"
  "ds_read_b64_tr_b16 a[28:29], v106 offset:8192\n"
  "ds_read_b64_tr_b16 a[30:31], v107 offset:8192\n"
  "ds_read_b64_tr_b16 a[48:49], v104 offset:16384\n"
  "ds_read_b64_tr_b16 a[50:51], v105 offset:16384\n"
  "ds_read_b64_tr_b16 a[52:53], v104 offset:24576\n"
  "ds_read_b64_tr_b16 a[54:55], v105 offset:24576\n"
  "ds_read_b64_tr_b16 a[56:57], v106 offset:16384\n"
  "ds_read_b64_tr_b16 a[58:59], v107 offset:16384\n"
  "ds_read_b64_tr_b16 a[60:61], v106 offset:24576\n"
  "ds_read_b64_tr_b16 a[62:63], v107 offset:24576\n"
  "ds_read_b64_tr_b16 a[80:81], v104 offset:32768\n"
  "ds_read_b64_tr_b16 a[82:83], v105 offset:32768\n"
  "ds_read_b64_tr_b16 a[84:85], v104 offset:40960\n"
  "ds_read_b64_tr_b16 a[86:87], v105 offset:40960\n"
  "ds_read_b64_tr_b16 a[88:89], v106 offset:32768\n"
  "ds_read_b64_tr_b16 a[90:91], v107 offset:32768\n"
  "ds_read_b64_tr_b16 a[92:93], v106 offset:40960\n"
  "ds_read_b64_tr_b16 a[94:95], v107 offset:40960\n"
  "ds_read_b64_tr_b16 a[112:113], v104 offset:49152\n"
  "ds_read_b64_tr_b16 a[114:115], v105 offset:49152\n"
  "ds_read_b64_tr_b16 a[116:117], v104 offset:57344\n"
  "ds_read_b64_tr_b16 a[118:119], v105 offset:57344\n"
  "ds_read_b64_tr_b16 a[120:121], v106 offset:49152\n"
  "ds_read_b64_tr_b16 a[122:123], v107 offset:49152\n"
  "ds_read_b64_tr_b16 a[124:125], v106 offset:57344\n"
  "ds_read_b64_tr_b16 a[126:127], v107 offset:57344\n"
  "v_xor_b32 v104, 0x10000, v104\n"
  "v_xor_b32 v105, 0x10000, v105\n"
  "v_xor_b32 v106, 0x10000, v106\n"
  "v_xor_b32 v107, 0x10000, v107\n"
  "v_xor_b32 v108, 0x10000, v108\n"
  "v_xor_b32 v109, 0x10000, v109\n"
  "v_xor_b32 v110, 0x10000, v110\n"
  "v_xor_b32 v111, 0x10000, v111\n"
  "s_waitcnt lgkmcnt(0)\n"
  "s_barrier\n"
  "s_add_u32 s76, s46, 0\n"
  "s_addc_u32 s77, s47, 0\n"
  "s_add_u32 m0, s75, 0\n"
  "s_nop 0\n"
  "global_load_lds_dwordx4 v100, s[76:77]\n"
  "s_add_u32 m0, s75, 1024\n"
  "s_nop 0\n"
  "global_load_lds_dwordx4 v101, s[76:77]\n"
  "s_add_u32 m0, s75, 2048\n"
  "s_nop 0\n"
  "global_load_lds_dwordx4 v102, s[76:77]\n"
  "s_add_u32 m0, s75, 3072\n"
  "s_nop 0\n"
  "global_load_lds_dwordx4 v103, s[76:77]\n"
  "s_add_u32 m0, s75, 16128\n"
  "s_nop 0\n"
  "global_load_lds_dwordx4 v100, s[76:77] offset:256\n"
  "s_add_u32 m0, s75, 17152\n"
  "s_nop 0\n"
  "global_load_lds_dwordx4 v101, s[76:77] offset:256\n"
  "s_add_u32 m0, s75, 18176\n"
  "s_nop 0\n"
  "global_load_lds_dwordx4 v102, s[76:77] offset:256\n"
  "s_add_u32 m0, s75, 19200\n"
  "s_nop 0\n"
  "global_load_lds_dwordx4 v103, s[76:77] offset:256\n"
  "s_add_u32 m0, s75, 32256\n"
  "s_nop 0\n"
  "global_load_lds_dwordx4 v100, s[76:77] offset:512\n"
  "s_add_u32 m0, s75, 33280\n"
  "s_nop 0\n"
  "global_load_lds_dwordx4 v101, s[76:77] offset:512\n"
  "s_add_u32 m0, s75, 34304\n"
  "s_nop 0\n"
  "global_load_lds_dwordx4 v102, s[76:77] offset:512\n"
  "s_add_u32 m0, s75, 35328\n"
  "s_nop 0\n"
  "global_load_lds_dwordx4 v103, s[76:77] offset:512\n"
  "s_add_u32 m0, s75, 48384\n"
  "s_nop 0\n"
  "global_load_lds_dwordx4 v100, s[76:77] offset:768\n"
  "s_add_u32 m0, s75, 49408\n"
  "s_nop 0\n"
  "global_load_lds_dwordx4 v101, s[76:77] offset:768\n"
  "s_add_u32 m0, s75, 50432\n"
  "s_nop 0\n"
  "global_load_lds_dwordx4 v102, s[76:77] offset:768\n"
  "s_add_u32 m0, s75, 51456\n"
  "s_nop 0\n"
  "global_load_lds_dwordx4 v103, s[76:77] offset:768\n"
  "s_waitcnt vmcnt(16)\n"
  "s_barrier\n"
  "ds_read_b64_tr_b16 a[144:145], v104\n"
  "ds_read_b64_tr_b16 a[146:147], v105\n"
  "ds_read_b64_tr_b16 a[148:149], v104 offset:8192\n"
  "ds_read_b64_tr_b16 a[150:151], v105 offset:8192\n"
  "ds_read_b64_tr_b16 a[152:153], v106\n"
  "ds_read_b64_tr_b16 a[154:155], v107\n"
  "ds_read_b64_tr_b16 a[156:157], v106 offset:8192\n"
  "ds_read_b64_tr_b16 a[158:159], v107 offset:8192\n"
  "ds_read_b64_tr_b16 a[176:177], v104 offset:16384\n"
  "ds_read_b64_tr_b16 a[178:179], v105 offset:16384\n"
  "ds_read_b64_tr_b16 a[180:181], v104 offset:24576\n"
  "ds_read_b64_tr_b16 a[182:183], v105 offset:24576\n"
  "ds_read_b64_tr_b16 a[184:185], v106 offset:16384\n"
  "ds_read_b64_tr_b16 a[186:187], v107 offset:16384\n"
  "ds_read_b64_tr_b16 a[188:189], v106 offset:24576\n"
  "ds_read_b64_tr_b16 a[190:191], v107 offset:24576\n"
  "ds_read_b64_tr_b16 a[208:209], v104 offset:32768\n"
  "ds_read_b64_tr_b16 a[210:211], v105 offset:32768\n"
  "ds_read_b64_tr_b16 a[212:213], v104 offset:40960\n"
  "ds_read_b64_tr_b16 a[214:215], v105 offset:40960\n"
  "ds_read_b64_tr_b16 a[216:217], v106 offset:32768\n"
  "ds_read_b64_tr_b16 a[218:219], v107 offset:32768\n"
  "ds_read_b64_tr_b16 a[220:221], v106 offset:40960\n"
  "ds_read_b64_tr_b16 a[222:223], v107 offset:40960\n"
  "ds_read_b64_tr_b16 a[240:241], v104 offset:49152\n"
  "ds_read_b64_tr_b16 a[242:243], v105 offset:49152\n"
  "ds_read_b64_tr_b16 a[244:245], v104 offset:57344\n"
  "ds_read_b64_tr_b16 a[246:247], v105 offset:57344\n"
  "ds_read_b64_tr_b16 a[248:249], v106 offset:49152\n"
  "ds_read_b64_tr_b16 a[250:251], v107 offset:49152\n"
  "ds_read_b64_tr_b16 a[252:253], v106 offset:57344\n"
  "ds_read_b64_tr_b16 a[254:255], v107 offset:57344\n"
  "v_xor_b32 v104, 0x10000, v104\n"
  "v_xor_b32 v105, 0x10000, v105\n"
  "v_xor_b32 v106, 0x10000, v106\n"
  "v_xor_b32 v107, 0x10000, v107\n"
  "v_xor_b32 v108, 0x10000, v108\n"
  "v_xor_b32 v109, 0x10000, v109\n"
  "v_xor_b32 v110, 0x10000, v110\n"
  "v_xor_b32 v111, 0x10000, v111\n"
  "s_waitcnt lgkmcnt(0)\n"
  "s_barrier\n"
  "s_add_u32 s76, s46, 65536\n"
  "s_addc_u32 s77, s47, 0\n"
  "s_add_u32 m0, s75, 65536\n"
  "s_nop 0\n"
  "global_load_lds_dwordx4 v100, s[76:77]\n"
  "s_add_u32 m0, s75, 66560\n"
  "s_nop 0\n"
  "global_load_lds_dwordx4 v101, s[76:77]\n"
  "s_add_u32 m0, s75, 67584\n"
  "s_nop 0\n"
  "global_load_lds_dwordx4 v102, s[76:77]\n"
  "s_add_u32 m0, s75, 68608\n"
  "s_nop 0\n"
  "global_load_lds_dwordx4 v103, s[76:77]\n"
  "s_add_u32 m0, s75, 81664\n"
  "s_nop 0\n"
  "global_load_lds_dwordx4 v100, s[76:77] offset:256\n"
  "s_add_u32 m0, s75, 82688\n"
  "s_nop 0\n"
  "global_load_lds_dwordx4 v101, s[76:77] offset:256\n"
  "s_add_u32 m0, s75, 83712\n"
  "s_nop 0\n"
  "global_load_lds_dwordx4 v102, s[76:77] offset:256\n"
  "s_add_u32 m0, s75, 84736\n"
  "s_nop 0\n"
  "global_load_lds_dwordx4 v103, s[76:77] offset:256\n"
  "s_add_u32 m0, s75, 97792\n"
  "s_nop 0\n"
  "global_load_lds_dwordx4 v100, s[76:77] offset:512\n"
  "s_add_u32 m0, s75, 98816\n"
  "s_nop 0\n"
  "global_load_lds_dwordx4 v101, s[76:77] offset:512\n"
  "s_add_u32 m0, s75, 99840\n"
  "s_nop 0\n"
  "global_load_lds_dwordx4 v102, s[76:77] offset:512\n"
  "s_add_u32 m0, s75, 100864\n"
  "s_nop 0\n"
  "global_load_lds_dwordx4 v103, s[76:77] offset:512\n"
  "s_add_u32 m0, s75, 113920\n"
  "s_nop 0\n"
  "global_load_lds_dwordx4 v100, s[76:77] offset:768\n"
  "s_add_u32 m0, s75, 114944\n"
  "s_nop 0\n"
  "global_load_lds_dwordx4 v101, s[76:77] offset:768\n"
  "s_add_u32 m0, s75, 115968\n"
  "s_nop 0\n"
  "global_load_lds_dwordx4 v102, s[76:77] offset:768\n"
  "s_add_u32 m0, s75, 116992\n"
  "s_nop 0\n"
  "global_load_lds_dwordx4 v103, s[76:77] offset:768\n"
  "s_waitcnt vmcnt(16)\n"
  "s_barrier\n"
  "v_xor_b32 v32, 0, v110\n"
  "v_xor_b32 v33, 0, v111\n"
  "ds_read_b64_tr_b16 v[128:129], v32\n"
  "ds_read_b64_tr_b16 v[130:131], v33\n"
  "ds_read_b64_tr_b16 v[132:133], v32 offset:8192\n"
  "ds_read_b64_tr_b16 v[134:135], v33 offset:8192\n"
  "v_xor_b32 v32, 32, v110\n"
  "v_xor_b32 v33, 32, v111\n"
  "ds_read_b64_tr_b16 v[144:145], v32\n"
  "ds_read_b64_tr_b16 v[146:147], v33\n"
  "ds_read_b64_tr_b16 v[148:149], v32 offset:8192\n"
  "ds_read_b64_tr_b16 v[150:151], v33 offset:8192\n"
  "v_xor_b32 v32, 64, v110\n"
  "v_xor_b32 v33, 64, v111\n"
  "ds_read_b64_tr_b16 v[160:161], v32\n"
  "ds_read_b64_tr_b16 v[162:163], v33\n"
  "ds_read_b64_tr_b16 v[164:165], v32 offset:8192\n"
  "ds_read_b64_tr_b16 v[166:167], v33 offset:8192\n"
  "v_xor_b32 v32, 96, v110\n"
  "v_xor_b32 v33, 96, v111\n"
  "ds_read_b64_tr_b16 v[176:177], v32\n"
  "ds_read_b64_tr_b16 v[178:179], v33\n"
  "ds_read_b64_tr_b16 v[180:181], v32 offset:8192\n"
  "ds_read_b64_tr_b16 v[182:183], v33 offset:8192\n"
  "v_xor_b32 v32, 128, v110\n"
  "v_xor_b32 v33, 128, v111\n"
  "ds_read_b64_tr_b16 v[192:193], v32\n"
  "ds_read_b64_tr_b16 v[194:195], v33\n"
  "ds_read_b64_tr_b16 v[196:197], v32 offset:8192\n"
  "ds_read_b64_tr_b16 v[198:199], v33 offset:8192\n"
  "v_xor_b32 v32, 160, v110\n"
  "v_xor_b32 v33, 160, v111\n"
  "ds_read_b64_tr_b16 v[208:209], v32\n"
  "ds_read_b64_tr_b16 v[210:211], v33\n"
  "ds_read_b64_tr_b16 v[212:213], v32 offset:8192\n"
  "ds_read_b64_tr_b16 v[214:215], v33 offset:8192\n"
  "v_xor_b32 v32, 192, v110\n"
  "v_xor_b32 v33, 192, v111\n"
  "ds_read_b64_tr_b16 v[224:225], v32\n"
  "ds_read_b64_tr_b16 v[226:227], v33\n"
  "ds_read_b64_tr_b16 v[228:229], v32 offset:8192\n"
  "ds_read_b64_tr_b16 v[230:231], v33 offset:8192\n"
  "v_xor_b32 v32, 224, v110\n"
  "v_xor_b32 v33, 224, v111\n"
  "ds_read_b64_tr_b16 v[240:241], v32\n"
  "ds_read_b64_tr_b16 v[242:243], v33\n"
  "ds_read_b64_tr_b16 v[244:245], v32 offset:8192\n"
  "ds_read_b64_tr_b16 v[246:247], v33 offset:8192\n"
  "v_xor_b32 v104, 0x10000, v104\n"
  "v_xor_b32 v105, 0x10000, v105\n"
  "v_xor_b32 v106, 0x10000, v106\n"
  "v_xor_b32 v107, 0x10000, v107\n"
  "v_xor_b32 v108, 0x10000, v108\n"
  "v_xor_b32 v109, 0x10000, v109\n"
  "v_xor_b32 v110, 0x10000, v110\n"
  "v_xor_b32 v111, 0x10000, v111\n"
  "s_waitcnt lgkmcnt(0)\n"
  "s_barrier\n"
  "s_add_u32 s76, s44, 0\n"
  "s_addc_u32 s77, s45, 0\n"
  "s_add_u32 m0, s78, 0\n"
  "s_nop 0\n"
  "global_load_lds_dwordx4 v96, s[76:77]\n"
  "s_add_u32 m0, s78, 1024\n"
  "s_nop 0\n"
  "global_load_lds_dwordx4 v97, s[76:77]\n"
  "s_add_u32 m0, s78, 2048\n"
  "s_nop 0\n"
  "global_load_lds_dwordx4 v98, s[76:77]\n"
  "s_add_u32 m0, s78, 3072\n"
  "s_nop 0\n"
  "global_load_lds_dwordx4 v99, s[76:77]\n"
  "s_add_u32 s76, s44, 131072\n"
  "s_addc_u32 s77, s45, 0\n"
  "s_add_u32 m0, s78, 4096\n"
  "s_nop 0\n"
  "global_load_lds_dwordx4 v96, s[76:77]\n"
  "s_add_u32 m0, s78, 5120\n"
  "s_nop 0\n"
  "global_load_lds_dwordx4 v97, s[76:77]\n"
  "s_add_u32 m0, s78, 6144\n"
  "s_nop 0\n"
  "global_load_lds_dwordx4 v98, s[76:77]\n"
  "s_add_u32 m0, s78, 7168\n"
  "s_nop 0\n"
  "global_load_lds_dwordx4 v99, s[76:77]\n"
  "s_add_u32 s76, s44, 262144\n"
  "s_addc_u32 s77, s45, 0\n"
  "s_add_u32 m0, s78, 8192\n"
  "s_nop 0\n"
  "global_load_lds_dwordx4 v96, s[76:77]\n"
  "s_add_u32 m0, s78, 9216\n"
  "s_nop 0\n"
  "global_load_lds_dwordx4 v97, s[76:77]\n"
  "s_add_u32 m0, s78, 10240\n"
  "s_nop 0\n"
  "global_load_lds_dwordx4 v98, s[76:77]\n"
  "s_add_u32 m0, s78, 11264\n"
  "s_nop 0\n"
  "global_load_lds_dwordx4 v99, s[76:77]\n"
  "s_add_u32 s76, s44, 393216\n"
  "s_addc_u32 s77, s45, 0\n"
  "s_add_u32 m0, s78, 12288\n"
  "s_nop 0\n"
  "global_load_lds_dwordx4 v96, s[76:77]\n"
  "s_add_u32 m0, s78, 13312\n"
  "s_nop 0\n"
  "global_load_lds_dwordx4 v97, s[76:77]\n"
  "s_add_u32 m0, s78, 14336\n"
  "s_nop 0\n"
  "global_load_lds_dwordx4 v98, s[76:77]\n"
  "s_add_u32 m0, s78, 15360\n"
  "s_nop 0\n"
  "global_load_lds_dwordx4 v99, s[76:77]\n"
  "s_waitcnt vmcnt(16)\n"
  "s_barrier\n"
  "v_xor_b32 v32, 0, v110\n"
  "v_xor_b32 v33, 0, v111\n"
  "ds_read_b64_tr_b16 v[136:137], v32\n"
  "ds_read_b64_tr_b16 v[138:139], v33\n"
  "ds_read_b64_tr_b16 v[140:141], v32 offset:8192\n"
  "ds_read_b64_tr_b16 v[142:143], v33 offset:8192\n"
  "v_xor_b32 v32, 32, v110\n"
  "v_xor_b32 v33, 32, v111\n"
  "ds_read_b64_tr_b16 v[152:153], v32\n"
  "ds_read_b64_tr_b16 v[154:155], v33\n"
  "ds_read_b64_tr_b16 v[156:157], v32 offset:8192\n"
  "ds_read_b64_tr_b16 v[158:159], v33 offset:8192\n"
  "v_xor_b32 v32, 64, v110\n"
  "v_xor_b32 v33, 64, v111\n"
  "ds_read_b64_tr_b16 v[168:169], v32\n"
  "ds_read_b64_tr_b16 v[170:171], v33\n"
  "ds_read_b64_tr_b16 v[172:173], v32 offset:8192\n"
  "ds_read_b64_tr_b16 v[174:175], v33 offset:8192\n"
  "v_xor_b32 v32, 96, v110\n"
  "v_xor_b32 v33, 96, v111\n"
  "ds_read_b64_tr_b16 v[184:185], v32\n"
  "ds_read_b64_tr_b16 v[186:187], v33\n"
  "ds_read_b64_tr_b16 v[188:189], v32 offset:8192\n"
  "ds_read_b64_tr_b16 v[190:191], v33 offset:8192\n"
  "v_xor_b32 v32, 128, v110\n"
  "v_xor_b32 v33, 128, v111\n"
  "ds_read_b64_tr_b16 v[200:201], v32\n"
  "ds_read_b64_tr_b16 v[202:203], v33\n"
  "ds_read_b64_tr_b16 v[204:205], v32 offset:8192\n"
  "ds_read_b64_tr_b16 v[206:207], v33 offset:8192\n"
  "v_xor_b32 v32, 160, v110\n"
  "v_xor_b32 v33, 160, v111\n"
  "ds_read_b64_tr_b16 v[216:217], v32\n"
  "ds_read_b64_tr_b16 v[218:219], v33\n"
  "ds_read_b64_tr_b16 v[220:221], v32 offset:8192\n"
  "ds_read_b64_tr_b16 v[222:223], v33 offset:8192\n"
  "v_xor_b32 v32, 192, v110\n"
  "v_xor_b32 v33, 192, v111\n"
  "ds_read_b64_tr_b16 v[232:233], v32\n"
  "ds_read_b64_tr_b16 v[234:235], v33\n"
  "ds_read_b64_tr_b16 v[236:237], v32 offset:8192\n"
  "ds_read_b64_tr_b16 v[238:239], v33 offset:8192\n"
  "v_xor_b32 v32, 224, v110\n"
  "v_xor_b32 v33, 224, v111\n"
  "ds_read_b64_tr_b16 v[248:249], v32\n"
  "ds_read_b64_tr_b16 v[250:251], v33\n"
  "ds_read_b64_tr_b16 v[252:253], v32 offset:8192\n"
  "ds_read_b64_tr_b16 v[254:255], v33 offset:8192\n"
  "v_xor_b32 v104, 0x10000, v104\n"
  "v_xor_b32 v105, 0x10000, v105\n"
  "v_xor_b32 v106, 0x10000, v106\n"
  "v_xor_b32 v107, 0x10000, v107\n"
  "v_xor_b32 v108, 0x10000, v108\n"
  "v_xor_b32 v109, 0x10000, v109\n"
  "v_xor_b32 v110, 0x10000, v110\n"
  "v_xor_b32 v111, 0x10000, v111\n"
  "s_waitcnt lgkmcnt(0)\n"
  "s_barrier\n"
  "s_add_u32 s76, s44, 2097152\n"
  "s_addc_u32 s77, s45, 0\n"
  "s_add_u32 m0, s78, 65536\n"
  "s_nop 0\n"
  "global_load_lds_dwordx4 v96, s[76:77]\n"
  "s_add_u32 m0, s78, 66560\n"
  "s_nop 0\n"
  "global_load_lds_dwordx4 v97, s[76:77]\n"
  "s_add_u32 m0, s78, 67584\n"
  "s_nop 0\n"
  "global_load_lds_dwordx4 v98, s[76:77]\n"
  "s_add_u32 m0, s78, 68608\n"
  "s_nop 0\n"
  "global_load_lds_dwordx4 v99, s[76:77]\n"
  "s_add_u32 s76, s44, 2228224\n"
  "s_addc_u32 s77, s45, 0\n"
  "s_add_u32 m0, s78, 69632\n"
  "s_nop 0\n"
  "global_load_lds_dwordx4 v96, s[76:77]\n"
  "s_add_u32 m0, s78, 70656\n"
  "s_nop 0\n"
  "global_load_lds_dwordx4 v97, s[76:77]\n"
  "s_add_u32 m0, s78, 71680\n"
  "s_nop 0\n"
  "global_load_lds_dwordx4 v98, s[76:77]\n"
  "s_add_u32 m0, s78, 72704\n"
  "s_nop 0\n"
  "global_load_lds_dwordx4 v99, s[76:77]\n"
  "s_add_u32 s76, s44, 2359296\n"
  "s_addc_u32 s77, s45, 0\n"
  "s_add_u32 m0, s78, 73728\n"
  "s_nop 0\n"
  "global_load_lds_dwordx4 v96, s[76:77]\n"
  "s_add_u32 m0, s78, 74752\n"
  "s_nop 0\n"
  "global_load_lds_dwordx4 v97, s[76:77]\n"
  "s_add_u32 m0, s78, 75776\n"
  "s_nop 0\n"
  "global_load_lds_dwordx4 v98, s[76:77]\n"
  "s_add_u32 m0, s78, 76800\n"
  "s_nop 0\n"
  "global_load_lds_dwordx4 v99, s[76:77]\n"
  "s_add_u32 s76, s44, 2490368\n"
  "s_addc_u32 s77, s45, 0\n"
  "s_add_u32 m0, s78, 77824\n"
  "s_nop 0\n"
  "global_load_lds_dwordx4 v96, s[76:77]\n"
  "s_add_u32 m0, s78, 78848\n"
  "s_nop 0\n"
  "global_load_lds_dwordx4 v97, s[76:77]\n"
  "s_add_u32 m0, s78, 79872\n"
  "s_nop 0\n"
  "global_load_lds_dwordx4 v98, s[76:77]\n"
  "s_add_u32 m0, s78, 80896\n"
  "s_nop 0\n"
  "global_load_lds_dwordx4 v99, s[76:77]\n"
  "s_waitcnt vmcnt(16)\n"
  "s_barrier\n"
  "ds_read_b64_tr_b16 v[32:33], v108\n"
  "ds_read_b64_tr_b16 v[34:35], v109\n"
  "ds_read_b64_tr_b16 v[36:37], v108 offset:8192\n"
  "ds_read_b64_tr_b16 v[38:39], v109 offset:8192\n"
  "ds_read_b64_tr_b16 v[40:41], v108 offset:8\n"
  "ds_read_b64_tr_b16 v[42:43], v109 offset:8\n"
  "ds_read_b64_tr_b16 v[44:45], v108 offset:8200\n"
  "ds_read_b64_tr_b16 v[46:47], v109 offset:8200\n"
  "ds_read_b64_tr_b16 v[48:49], v108 offset:16384\n"
  "ds_read_b64_tr_b16 v[50:51], v109 offset:16384\n"
  "ds_read_b64_tr_b16 v[52:53], v108 offset:24576\n"
  "ds_read_b64_tr_b16 v[54:55], v109 offset:24576\n"
  "ds_read_b64_tr_b16 v[56:57], v108 offset:16392\n"
  "ds_read_b64_tr_b16 v[58:59], v109 offset:16392\n"
  "ds_read_b64_tr_b16 v[60:61], v108 offset:24584\n"
  "ds_read_b64_tr_b16 v[62:63], v109 offset:24584\n"
  "ds_read_b64_tr_b16 v[64:65], v108 offset:32768\n"
  "ds_read_b64_tr_b16 v[66:67], v109 offset:32768\n"
  "ds_read_b64_tr_b16 v[68:69], v108 offset:40960\n"
  "ds_read_b64_tr_b16 v[70:71], v109 offset:40960\n"
  "ds_read_b64_tr_b16 v[72:73], v108 offset:32776\n"
  "ds_read_b64_tr_b16 v[74:75], v109 offset:32776\n"
  "ds_read_b64_tr_b16 v[76:77], v108 offset:40968\n"
  "ds_read_b64_tr_b16 v[78:79], v109 offset:40968\n"
  "ds_read_b64_tr_b16 v[80:81], v108 offset:49152\n"
  "ds_read_b64_tr_b16 v[82:83], v109 offset:49152\n"
  "ds_read_b64_tr_b16 v[84:85], v108 offset:57344\n"
  "ds_read_b64_tr_b16 v[86:87], v109 offset:57344\n"
  "ds_read_b64_tr_b16 v[88:89], v108 offset:49160\n"
  "ds_read_b64_tr_b16 v[90:91], v109 offset:49160\n"
  "ds_read_b64_tr_b16 v[92:93], v108 offset:57352\n"
  "ds_read_b64_tr_b16 v[94:95], v109 offset:57352\n"
  "s_waitcnt lgkmcnt(0)\n"
  "s_barrier\n"
  "ds_write_b128 v2, v[32:35]\n"
  "ds_write_b128 v2, v[36:39] offset:1024\n"
  "ds_write_b128 v2, v[40:43] offset:2048\n"
  "ds_write_b128 v2, v[44:47] offset:3072\n"
  "ds_write_b128 v2, v[48:51] offset:4096\n"
  "ds_write_b128 v2, v[52:55] offset:5120\n"
  "ds_write_b128 v2, v[56:59] offset:6144\n"
  "ds_write_b128 v2, v[60:63] offset:7168\n"
  "ds_write_b128 v2, v[64:67] offset:8192\n"
  "ds_write_b128 v2, v[68:71] offset:9216\n"
  "ds_write_b128 v2, v[72:75] offset:10240\n"
  "ds_write_b128 v2, v[76:79] offset:11264\n"
  "ds_write_b128 v2, v[80:83] offset:12288\n"
  "ds_write_b128 v2, v[84:87] offset:13312\n"
  "ds_write_b128 v2, v[88:91] offset:14336\n"
  "ds_write_b128 v2, v[92:95] offset:15360\n"
  "v_xor_b32 v104, 0x10000, v104\n"
  "v_xor_b32 v105, 0x10000, v105\n"
  "v_xor_b32 v106, 0x10000, v106\n"
  "v_xor_b32 v107, 0x10000, v107\n"
  "v_xor_b32 v108, 0x10000, v108\n"
  "v_xor_b32 v109, 0x10000, v109\n"
  "v_xor_b32 v110, 0x10000, v110\n"
  "v_xor_b32 v111, 0x10000, v111\n"
  "s_waitcnt lgkmcnt(0)\n"
  "s_barrier\n"
  "s_waitcnt vmcnt(0)\n"
  "s_barrier\n"
  "ds_read_b64_tr_b16 v[32:33], v108\n"
  "ds_read_b64_tr_b16 v[34:35], v109\n"
  "ds_read_b64_tr_b16 v[36:37], v108 offset:8192\n"
  "ds_read_b64_tr_b16 v[38:39], v109 offset:8192\n"
  "ds_read_b64_tr_b16 v[40:41], v108 offset:8\n"
  "ds_read_b64_tr_b16 v[42:43], v109 offset:8\n"
  "ds_read_b64_tr_b16 v[44:45], v108 offset:8200\n"
  "ds_read_b64_tr_b16 v[46:47], v109 offset:8200\n"
  "ds_read_b64_tr_b16 v[48:49], v108 offset:16384\n"
  "ds_read_b64_tr_b16 v[50:51], v109 offset:16384\n"
  "ds_read_b64_tr_b16 v[52:53], v108 offset:24576\n"
  "ds_read_b64_tr_b16 v[54:55], v109 offset:24576\n"
  "ds_read_b64_tr_b16 v[56:57], v108 offset:16392\n"
  "ds_read_b64_tr_b16 v[58:59], v109 offset:16392\n"
  "ds_read_b64_tr_b16 v[60:61], v108 offset:24584\n"
  "ds_read_b64_tr_b16 v[62:63], v109 offset:24584\n"
  "ds_read_b64_tr_b16 v[64:65], v108 offset:32768\n"
  "ds_read_b64_tr_b16 v[66:67], v109 offset:32768\n"
  "ds_read_b64_tr_b16 v[68:69], v108 offset:40960\n"
  "ds_read_b64_tr_b16 v[70:71], v109 offset:40960\n"
  "ds_read_b64_tr_b16 v[72:73], v108 offset:32776\n"
  "ds_read_b64_tr_b16 v[74:75], v109 offset:32776\n"
  "ds_read_b64_tr_b16 v[76:77], v108 offset:40968\n"
  "ds_read_b64_tr_b16 v[78:79], v109 offset:40968\n"
  "ds_read_b64_tr_b16 v[80:81], v108 offset:49152\n"
  "ds_read_b64_tr_b16 v[82:83], v109 offset:49152\n"
  "ds_read_b64_tr_b16 v[84:85], v108 offset:57344\n"
  "ds_read_b64_tr_b16 v[86:87], v109 offset:57344\n"
  "ds_read_b64_tr_b16 v[88:89], v108 offset:49160\n"
  "ds_read_b64_tr_b16 v[90:91], v109 offset:49160\n"
  "ds_read_b64_tr_b16 v[92:93], v108 offset:57352\n"
  "ds_read_b64_tr_b16 v[94:95], v109 offset:57352\n"
  "s_waitcnt lgkmcnt(0)\n"
  "s_barrier\n"
  "ds_write_b128 v3, v[32:35]\n"
  "ds_write_b128 v3, v[36:39] offset:1024\n"
  "ds_write_b128 v3, v[40:43] offset:2048\n"
  "ds_write_b128 v3, v[44:47] offset:3072\n"
  "ds_write_b128 v3, v[48:51] offset:4096\n"
  "ds_write_b128 v3, v[52:55] offset:5120\n"
  "ds_write_b128 v3, v[56:59] offset:6144\n"
  "ds_write_b128 v3, v[60:63] offset:7168\n"
  "ds_write_b128 v3, v[64:67] offset:8192\n"
  "ds_write_b128 v3, v[68:71] offset:9216\n"
  "ds_write_b128 v3, v[72:75] offset:10240\n"
  "ds_write_b128 v3, v[76:79] offset:11264\n"
  "ds_write_b128 v3, v[80:83] offset:12288\n"
  "ds_write_b128 v3, v[84:87] offset:13312\n"
  "ds_write_b128 v3, v[88:91] offset:14336\n"
  "ds_write_b128 v3, v[92:95] offset:15360\n"
  "v_xor_b32 v104, 0x10000, v104\n"
  "v_xor_b32 v105, 0x10000, v105\n"
  "v_xor_b32 v106, 0x10000, v106\n"
  "v_xor_b32 v107, 0x10000, v107\n"
  "v_xor_b32 v108, 0x10000, v108\n"
  "v_xor_b32 v109, 0x10000, v109\n"
  "v_xor_b32 v110, 0x10000, v110\n"
  "v_xor_b32 v111, 0x10000, v111\n"
  "s_waitcnt lgkmcnt(0)\n"
  "s_barrier\n"
  "s_add_u32 s76, s56, 0x320000\n"
  "s_addc_u32 s77, s57, 0\n"
  "v_mov_b32 v32, 0\n"
  "s_mov_b32 s73, 200000\n"
  "CH_par_%=:\n"
  "global_load_dword v33, v32, s[76:77] offset:512 sc1\n"
  "s_waitcnt vmcnt(0)\n"
  "v_readfirstlane_b32 s74, v33\n"
  "s_cmp_eq_u32 s74, -1\n"
  "s_cbranch_scc0 CH_parok_%=\n"
  "s_sleep 2\n"
  "s_sub_u32 s73, s73, 1\n"
  "s_cbranch_scc0 CH_par_%=\n"
  "s_branch CH_END_%=\n"
  "CH_parok_%=:\n"
  "s_lshl_b32 s75, s74, 13\n"
  "s_add_u32 s56, s56, s75\n"
  "s_addc_u32 s57, s57, 0\n"
  "s_lshl_b32 s75, s74, 14\n"
  "s_sub_u32 s99, 0x202000, s75\n"
  "s_lshl_b32 s75, s69, 9\n"
  "s_add_u32 s75, s75, 4096\n"
  "s_add_u32 s76, s76, s75\n"
  "s_addc_u32 s77, s77, 0\n"
  "s_add_u32 s63, s75, 0x3200fc\n"
  "s_lshl_b32 s75, s74, 13\n"
  "s_sub_u32 s63, s63, s75\n"
  "s_mov_b32 s78, 0\n"
  "s_cmp_gt_u32 s78, 1\n"
  "s_cbranch_scc1 CH_kv0_%=_ok\n"
  "s_mov_b32 s73, 200000\n"
  "v_lshrrev_b32 v32, 2, v7\n"
  "CH_kv0_%=_p:\n"
  "global_load_dword v33, v32, s[76:77] sc1\n"
  "s_waitcnt vmcnt(0)\n"
  "v_cmp_eq_u32 vcc, -1, v33\n"
  "s_ff1_i32_b64 s78, vcc\n"
  "s_cmp_lt_i32 s78, 0\n"
  "s_cselect_b32 s78, 64, s78\n"
  "s_cmp_gt_u32 s78, 1\n"
  "s_cbranch_scc1 CH_kv0_%=_ok\n"
  "s_sleep 2\n"
  "s_sub_u32 s73, s73, 1\n"
  "s_cbranch_scc0 CH_kv0_%=_p\n"
  "s_branch CH_END_%=\n"
  "CH_kv0_%=_ok:\n"
  "s_cmp_eq_u32 s68, 1\n"
  "s_cbranch_scc1 CH_start_1_%=\n"
  "s_cmp_eq_u32 s68, 2\n"
  "s_cbranch_scc1 CH_start_2_%=\n"
  "s_cmp_eq_u32 s68, 3\n"
  "s_cbranch_scc1 CH_start_3_%=\n"
  "CH_start_0_%=:\n"
  "s_add_u32 s60, s60, 0\n"
  "s_addc_u32 s61, s61, 0\n"
  "s_mov_b32 s64, 0\n"
  "s_mov_b32 s65, 2048\n"
  "s_mov_b32 s62, 0\n"
  "s_lshl_b32 s74, s69, 8\n"
  "s_add_u32 s66, s74, 0\n"
  "s_lshl_b32 s88, s69, 14\n"
  "s_add_u32 s90, s88, 1572864\n"
  "s_add_u32 s67, s99, 16384\n"
  "s_add_u32 s91, s74, s99\n"
  "s_add_u32 s91, s91, 16384\n"
  "v_add_u32 v22, 2304, v4\n"
  "s_add_u32 s50, s60, 8192\n"
  "s_addc_u32 s51, s61, 0\n"
  "s_mov_b32 m0, 139264\n"
  "s_nop 0\n"
  "global_load_lds_dwordx4 v7, s[60:61] sc1\n"
  "global_load_lds_dwordx4 v7, s[60:61] offset:1024 sc1\n"
  "s_mov_b32 m0, 147456\n"
  "s_nop 0\n"
  "global_load_lds_dwordx4 v7, s[50:51] sc1\n"
  "global_load_lds_dwordx4 v7, s[50:51] offset:1024 sc1\n"
  "s_mov_b32 s74, 524288\n"
  "s_add_u32 s50, s50, s74\n"
  "s_addc_u32 s51, s51, 0\n"
  "s_add_u32 s60, s60, s74\n"
  "s_addc_u32 s61, s61, 0\n"
  "global_load_dwordx4 v[32:35], v112, s[80:81]\n"
  "global_load_dwordx4 v[36:39], v112, s[80:81] offset:128\n"
  "ds_read_b128 v[88:91], v2\n"
  "ds_read_b128 v[92:95], v2 offset:1024\n"
  "ds_read_b128 v[96:99], v2 offset:2048\n"
  "ds_read_b128 v[100:103], v2 offset:3072\n"
  "ds_read_b128 v[104:107], v2 offset:4096\n"
  "ds_read_b128 v[108:111], v2 offset:5120\n"
  "ds_read_b128 v[112:115], v2 offset:6144\n"
  "ds_read_b128 v[116:119], v2 offset:7168\n"
  "v_mov_b64 v[64:65], 0\n"
  "v_mov_b64 v[66:67], 0\n"
  "v_mov_b64 v[68:69], 0\n"
  "v_mov_b64 v[70:71], 0\n"
  "v_mov_b64 v[72:73], 0\n"
  "v_mov_b64 v[74:75], 0\n"
  "v_mov_b64 v[76:77], 0\n"
  "v_mov_b64 v[78:79], 0\n"
  "v_mov_b64 v[80:81], 0\n"
  "v_mov_b64 v[82:83], 0\n"
  "v_mov_b64 v[84:85], 0\n"
  "v_mov_b64 v[86:87], 0\n"
  "s_waitcnt vmcnt(0) lgkmcnt(0)\n"
  "s_barrier\n"
  "s_branch CH_loop_0_%=\n"
  "CH_p2_0_%=:\n"
  "buffer_load_dwordx4 v[32:35], v7, s[56:59], s67 offen sc1\n"
  "buffer_load_dwordx4 v[36:39], v7, s[56:59], s67 offen offset:1024 sc1\n"
  "s_sleep 5\n"
  "buffer_load_dwordx4 v[120:123], v7, s[56:59], s67 offen sc1\n"
  "buffer_load_dwordx4 v[124:127], v7, s[56:59], s67 offen offset:1024 sc1\n"
  "s_waitcnt vmcnt(2)\n"
  "v_max3_u32 v48, v32, v34, v36\n"
  "v_max_u32 v48, v48, v38\n"
  "v_cmp_eq_u32 vcc, -1, v48\n"
  "s_cbranch_vccnz CH_p2retry_0_%=\n"
  "CH_p2ok_0_%=:\n"
  "s_add_u32 s67, s67, 16384\n"
  "CH_loop_0_%=:\n"
  "ds_write_b128 v1, v[32:35]\n"
  "ds_write_b128 v1, v[36:39] offset:1024\n"
  "v_smfmac_f32_16x16x64_bf16 v[64:67], v[32:35], a[0:7], v0\n"
  "v_smfmac_f32_16x16x64_bf16 v[68:71], v[32:35], a[8:15], v0\n"
  "v_smfmac_f32_16x16x64_bf16 v[64:67], v[36:39], a[32:39], v0\n"
  "v_smfmac_f32_16x16x64_bf16 v[68:71], v[36:39], a[40:47], v0\n"
  "s_waitcnt lgkmcnt(0)\n"
  "s_barrier\n"
  "ds_read_b128 v[40:43], v1 offset:2048\n"
  "ds_read_b128 v[44:47], v1 offset:3072\n"
  "ds_read_b128 v[48:51], v1 offset:4096\n"
  "ds_read_b128 v[52:55], v1 offset:5120\n"
  "v_smfmac_f32_16x16x64_bf16 v[80:83], v[32:35], v[88:95], v0\n"
  "ds_read_b128 v[88:91], v2 offset:8192\n"
  "ds_read_b128 v[92:95], v2 offset:9216\n"
  "v_smfmac_f32_16x16x64_bf16 v[84:87], v[32:35], v[96:103], v0\n"
  "ds_read_b128 v[96:99], v2 offset:10240\n"
  "ds_read_b128 v[100:103], v2 offset:11264\n"
  "v_smfmac_f32_16x16x64_bf16 v[80:83], v[36:39], v[104:111], v0\n"
  "ds_read_b128 v[104:107], v2 offset:12288\n"
  "ds_read_b128 v[108:111], v2 offset:13312\n"
  "v_smfmac_f32_16x16x64_bf16 v[84:87], v[36:39], v[112:119], v0\n"
  "ds_read_b128 v[112:115], v2 offset:14336\n"
  "ds_read_b128 v[116:119], v2 offset:15360\n"
  "s_waitcnt vmcnt(0)\n"
  "ds_read_b128 v[56:59], v1 offset:6144\n"
  "ds_read_b128 v[60:63], v1 offset:7168\n"
  "v_smfmac_f32_16x16x64_bf16 v[72:75], v[32:35], a[16:23], v0\n"
  "v_smfmac_f32_16x16x64_bf16 v[76:79], v[32:35], a[24:31], v0\n"
  "v_smfmac_f32_16x16x64_bf16 v[72:75], v[36:39], a[48:55], v0\n"
  "v_smfmac_f32_16x16x64_bf16 v[76:79], v[36:39], a[56:63], v0\n"
  "s_waitcnt lgkmcnt(10)\n"
  "v_smfmac_f32_16x16x64_bf16 v[64:67], v[40:43], a[64:71], v0\n"
  "v_smfmac_f32_16x16x64_bf16 v[68:71], v[40:43], a[72:79], v0\n"
  "v_smfmac_f32_16x16x64_bf16 v[64:67], v[44:47], a[96:103], v0\n"
  "v_smfmac_f32_16x16x64_bf16 v[68:71], v[44:47], a[104:111], v0\n"
  "ds_read_b128 v[32:35], v1 offset:8192\n"
  "v_smfmac_f32_16x16x64_bf16 v[64:67], v[48:51], a[128:135], v0\n"
  "v_smfmac_f32_16x16x64_bf16 v[68:71], v[48:51], a[136:143], v0\n"
  "ds_read_b128 v[36:39], v1 offset:12288\n"
  "v_smfmac_f32_16x16x64_bf16 v[64:67], v[52:55], a[160:167], v0\n"
  "v_smfmac_f32_16x16x64_bf16 v[68:71], v[52:55], a[168:175], v0\n"
  "ds_read_b128 v[120:123], v1 offset:9216\n"
  "s_waitcnt lgkmcnt(3)\n"
  "v_smfmac_f32_16x16x64_bf16 v[64:67], v[56:59], a[192:199], v0\n"
  "v_smfmac_f32_16x16x64_bf16 v[68:71], v[56:59], a[200:207], v0\n"
  "ds_read_b128 v[124:127], v1 offset:13312\n"
  "v_smfmac_f32_16x16x64_bf16 v[64:67], v[60:63], a[224:231], v0\n"
  "v_smfmac_f32_16x16x64_bf16 v[68:71], v[60:63], a[232:239], v0\n"
  "v_smfmac_f32_16x16x64_bf16 v[72:75], v[40:43], a[80:87], v0\n"
  "v_smfmac_f32_16x16x64_bf16 v[76:79], v[40:43], a[88:95], v0\n"
  "v_pk_add_f32 v[64:65], v[64:65], v[66:67]\n"
  "v_pk_add_f32 v[68:69], v[68:69], v[70:71]\n"
  "v_smfmac_f32_16x16x64_bf16 v[80:83], v[40:43], v[88:95], v0\n"
  "ds_read_b128 v[88:91], v3\n"
  "ds_read_b128 v[92:95], v3 offset:1024\n"
  "v_cvt_pk_bf16_f32 v64, v64, v68\n"
  "v_cvt_pk_bf16_f32 v65, v65, v69\n"
  "v_smfmac_f32_16x16x64_bf16 v[84:87], v[40:43], v[96:103], v0\n"
  "ds_read_b128 v[96:99], v3 offset:2048\n"
  "ds_read_b128 v[100:103], v3 offset:3072\n"
  "ds_write2_b32 v4, v64, v65 offset0:0 offset1:72\n"
  "v_smfmac_f32_16x16x64_bf16 v[72:75], v[44:47], a[112:119], v0\n"
  "v_smfmac_f32_16x16x64_bf16 v[76:79], v[44:47], a[120:127], v0\n"
  "v_smfmac_f32_16x16x64_bf16 v[80:83], v[44:47], v[104:111], v0\n"
  "ds_read_b128 v[104:107], v3 offset:4096\n"
  "ds_read_b128 v[108:111], v3 offset:5120\n"
  "v_smfmac_f32_16x16x64_bf16 v[84:87], v[44:47], v[112:119], v0\n"
  "ds_read_b128 v[112:115], v3 offset:6144\n"
  "ds_read_b128 v[116:119], v3 offset:7168\n"
  "v_smfmac_f32_16x16x64_bf16 v[72:75], v[48:51], a[144:151], v0\n"
  "s_waitcnt lgkmcnt(4)\n"
  "s_barrier\n"
  "ds_read_b128 v[64:67], v5\n"
  "ds_read_b128 v[68:71], v5 offset:64\n"
  "ds_read_b128 v[40:43], v5 offset:128\n"
  "ds_read_b128 v[44:47], v5 offset:192\n"
  "v_smfmac_f32_16x16x64_bf16 v[76:79], v[48:51], a[152:159], v0\n"
  "v_smfmac_f32_16x16x64_bf16 v[80:83], v[48:51], v[88:95], v0\n"
  "ds_read_b128 v[88:91], v3 offset:8192\n"
  "ds_read_b128 v[92:95], v3 offset:9216\n"
  "v_smfmac_f32_16x16x64_bf16 v[84:87], v[48:51], v[96:103], v0\n"
  "ds_read_b128 v[96:99], v3 offset:10240\n"
  "ds_read_b128 v[100:103], v3 offset:11264\n"
  "ds_read_b128 v[48:51], v1 offset:10240\n"
  "v_smfmac_f32_16x16x64_bf16 v[72:75], v[52:55], a[176:183], v0\n"
  "v_smfmac_f32_16x16x64_bf16 v[76:79], v[52:55], a[184:191], v0\n"
  "s_waitcnt lgkmcnt(11)\n"
  "v_smfmac_f32_16x16x64_bf16 v[80:83], v[52:55], v[104:111], v0\n"
  "s_waitcnt lgkmcnt(9)\n"
  "v_smfmac_f32_16x16x64_bf16 v[84:87], v[52:55], v[112:119], v0\n"
  "ds_read_b128 v[52:55], v1 offset:14336\n"
  "ds_read_b128 v[104:107], v1 offset:11264\n"
  "ds_read_b128 v[112:115], v1 offset:15360\n"
  "v_smfmac_f32_16x16x64_bf16 v[72:75], v[56:59], a[208:215], v0\n"
  "v_smfmac_f32_16x16x64_bf16 v[76:79], v[56:59], a[216:223], v0\n"
  "v_smfmac_f32_16x16x64_bf16 v[72:75], v[60:63], a[240:247], v0\n"
  "v_smfmac_f32_16x16x64_bf16 v[76:79], v[60:63], a[248:255], v0\n"
  "s_waitcnt lgkmcnt(6)\n"
  "v_mfma_f32_16x16x32_bf16 v[32:35], v[32:35], v[64:67], 0\n"
  "v_mfma_f32_16x16x32_bf16 v[36:39], v[36:39], v[64:67], 0\n"
  "v_mfma_f32_16x16x32_bf16 v[32:35], v[120:123], v[68:71], v[32:35]\n"
  "v_pk_add_f32 v[72:73], v[72:73], v[74:75]\n"
  "v_pk_add_f32 v[76:77], v[76:77], v[78:79]\n"
  "v_mfma_f32_16x16x32_bf16 v[36:39], v[124:127], v[68:71], v[36:39]\n"
  "v_cvt_pk_bf16_f32 v72, v72, v76\n"
  "v_cvt_pk_bf16_f32 v73, v73, v77\n"
  "s_waitcnt lgkmcnt(3)\n"
  "v_mfma_f32_16x16x32_bf16 v[32:35], v[48:51], v[40:43], v[32:35]\n"
  "ds_write2_b32 v22, v72, v73 offset0:0 offset1:72\n"
  "s_waitcnt lgkmcnt(3)\n"
  "v_mfma_f32_16x16x32_bf16 v[36:39], v[52:55], v[40:43], v[36:39]\n"
  "s_waitcnt lgkmcnt(2)\n"
  "v_mfma_f32_16x16x32_bf16 v[32:35], v[104:107], v[44:47], v[32:35]\n"
  "s_waitcnt lgkmcnt(1)\n"
  "v_mfma_f32_16x16x32_bf16 v[36:39], v[112:115], v[44:47], v[36:39]\n"
  "v_smfmac_f32_16x16x64_bf16 v[80:83], v[56:59], v[88:95], v0\n"
  "ds_read_b128 v[88:91], v3 offset:12288\n"
  "ds_read_b128 v[92:95], v3 offset:13312\n"
  "v_smfmac_f32_16x16x64_bf16 v[84:87], v[56:59], v[96:103], v0\n"
  "ds_read_b128 v[96:99], v3 offset:14336\n"
  "ds_read_b128 v[100:103], v3 offset:15360\n"
  "s_waitcnt lgkmcnt(4)\n"
  "s_barrier\n"
  "ds_read_b128 v[72:75], v5 offset:2304\n"
  "ds_read_b128 v[76:79], v5 offset:2368\n"
  "ds_read_b128 v[120:123], v5 offset:2432\n"
  "ds_read_b128 v[124:127], v5 offset:2496\n"
  "ds_read_b128 v[48:51], v1 offset:16384\n"
  "ds_read_b128 v[52:55], v1 offset:17408\n"
  "s_mov_b32 m0, 139264\n"
  "s_nop 0\n"
  "global_load_lds_dwordx4 v7, s[60:61] sc1\n"
  "global_load_lds_dwordx4 v7, s[60:61] offset:1024 sc1\n"
  "v_max3_f32 v104, v32, v33, v34\n"
  "v_max3_f32 v105, v35, v36, v37\n"
  "v_max3_f32 v104, v104, v38, v39\n"
  "v_max_f32 v104, v104, v105\n"
  "s_waitcnt lgkmcnt(8)\n"
  "v_smfmac_f32_16x16x64_bf16 v[80:83], v[60:63], v[88:95], v0\n"
  "s_waitcnt lgkmcnt(6)\n"
  "v_smfmac_f32_16x16x64_bf16 v[84:87], v[60:63], v[96:103], v0\n"
  "s_waitcnt lgkmcnt(5)\n"
  "v_mfma_f32_16x16x32_bf16 v[112:115], v[72:75], v[64:67], 0\n"
  "s_waitcnt lgkmcnt(4)\n"
  "v_mfma_f32_16x16x32_bf16 v[112:115], v[76:79], v[68:71], v[112:115]\n"
  "s_waitcnt lgkmcnt(3)\n"
  "v_mfma_f32_16x16x32_bf16 v[112:115], v[120:123], v[40:43], v[112:115]\n"
  "s_waitcnt lgkmcnt(2)\n"
  "v_mfma_f32_16x16x32_bf16 v[112:115], v[124:127], v[44:47], v[112:115]\n"
  "s_mov_b64 exec, s[70:71]\n"
  "buffer_store_dwordx4 v[72:75], v10, s[52:55], s64 offen sc1\n"
  "s_mov_b64 exec, -1\n"
  "s_nop 3\n"
  "v_max3_f32 v104, v104, v112, v113\n"
  "v_mov_b32 v105, v104\n"
  "s_nop 1\n"
  "v_permlane32_swap_b32 v104, v105\n"
  "v_max_f32 v104, v104, v105\n"
  "v_mov_b32 v105, v104\n"
  "s_nop 1\n"
  "v_permlane16_swap_b32 v104, v105\n"
  "v_max_f32 v104, v104, v105\n"
  "v_mul_f32 v104, s72, v104\n"
  "v_pk_fma_f32 v[32:33], v[32:33], s[72:73], v[104:105] op_sel_hi:[1,0,0] neg_lo:[0,0,1] neg_hi:[0,0,1]\n"
  "v_pk_fma_f32 v[34:35], v[34:35], s[72:73], v[104:105] op_sel_hi:[1,0,0] neg_lo:[0,0,1] neg_hi:[0,0,1]\n"
  "v_pk_fma_f32 v[36:37], v[36:37], s[72:73], v[104:105] op_sel_hi:[1,0,0] neg_lo:[0,0,1] neg_hi:[0,0,1]\n"
  "v_pk_fma_f32 v[38:39], v[38:39], s[72:73], v[104:105] op_sel_hi:[1,0,0] neg_lo:[0,0,1] neg_hi:[0,0,1]\n"
  "v_exp_f32 v32, v32\n"
  "v_exp_f32 v33, v33\n"
  "v_exp_f32 v34, v34\n"
  "v_exp_f32 v35, v35\n"
  "v_exp_f32 v36, v36\n"
  "v_exp_f32 v37, v37\n"
  "v_exp_f32 v38, v38\n"
  "v_exp_f32 v39, v39\n"
  "v_cvt_pk_bf16_f32 v40, v32, v33\n"
  "v_cvt_pk_bf16_f32 v41, v34, v35\n"
  "v_cvt_pk_bf16_f32 v42, v36, v37\n"
  "v_cvt_pk_bf16_f32 v43, v38, v39\n"
  "v_pk_fma_f32 v[112:113], v[112:113], s[72:73], v[104:105] op_sel_hi:[1,0,0] neg_lo:[0,0,1] neg_hi:[0,0,1]\n"
  "s_nop 0\n"
  "v_mfma_f32_16x16x32_bf16 v[44:47], v[18:21], v[40:43], 0\n"
  "v_exp_f32 v112, v112\n"
  "v_exp_f32 v113, v113\n"
  "s_waitcnt lgkmcnt(0)\n"
  "v_mfma_f32_16x16x32_bf16 v[64:67], v[48:51], v[40:43], 0\n"
  "v_cvt_pk_bf16_f32 v24, v80, v81\n"
  "v_cvt_pk_bf16_f32 v25, v82, v83\n"
  "v_mfma_f32_16x16x32_bf16 v[68:71], v[52:55], v[40:43], 0\n"
  "v_cvt_pk_bf16_f32 v28, v112, v113\n"
  "v_cvt_pk_bf16_f32 v29, v112, v113\n"
  "v_cvt_pk_bf16_f32 v26, v84, v85\n"
  "v_cvt_pk_bf16_f32 v27, v86, v87\n"
  "v_mfma_f32_16x16x32_bf16 v[44:47], v[16:19], v[28:31], v[44:47]\n"
  "v_mfma_f32_16x16x32_bf16 v[64:67], v[24:27], v[28:31], v[64:67]\n"
  "v_mfma_f32_16x16x32_bf16 v[68:71], v[26:29], v[28:31], v[68:71]\n"
  "v_mov_b64 v[32:33], 0\n"
  "v_mov_b64 v[34:35], 0\n"
  "v_mov_b64 v[36:37], 0\n"
  "v_mov_b64 v[38:39], 0\n"
  "v_rcp_f32 v106, v44\n"
  "v_mov_b64 v[56:57], 0\n"
  "v_pk_mul_f32 v[64:65], v[64:65], v[106:107] op_sel_hi:[1,0]\n"
  "v_pk_mul_f32 v[66:67], v[66:67], v[106:107] op_sel_hi:[1,0]\n"
  "v_pk_mul_f32 v[68:69], v[68:69], v[106:107] op_sel_hi:[1,0]\n"
  "v_pk_mul_f32 v[70:71], v[70:71], v[106:107] op_sel_hi:[1,0]\n"
  "v_cvt_pk_bf16_f32 v84, v64, v65\n"
  "v_cvt_pk_bf16_f32 v85, v66, v67\n"
  "v_cvt_pk_bf16_f32 v86, v68, v69\n"
  "v_cvt_pk_bf16_f32 v87, v70, v71\n"
  "ds_write_b128 v6, v[84:87]\n"
  "buffer_store_dwordx2 v[24:25], v11, s[52:55], s65 offen sc1\n"
  "buffer_store_dwordx2 v[26:27], v11, s[52:55], s65 offen offset:512 sc1\n"
  "s_cmp_eq_u32 s62, 63\n"
  "s_cbranch_scc1 CH_exit_0_%=\n"
  "v_mov_b64 v[58:59], 0\n"
  "v_mov_b64 v[60:61], 0\n"
  "v_mov_b64 v[62:63], 0\n"
  "s_waitcnt lgkmcnt(0)\n"
  "s_barrier\n"
  "ds_read_b128 v[88:91], v1 offset:29184\n"
  "ds_read_b128 v[92:95], v1 offset:30208\n"
  "v_mov_b64 v[40:41], 0\n"
  "v_mov_b64 v[42:43], 0\n"
  "v_mov_b64 v[44:45], 0\n"
  "v_mov_b64 v[46:47], 0\n"
  "v_mov_b64 v[48:49], 0\n"
  "v_mov_b64 v[50:51], 0\n"
  "v_mov_b64 v[52:53], 0\n"
  "v_mov_b64 v[54:55], 0\n"
  "s_add_u32 s84, s66, 0\n"
  "s_add_u32 s85, s66, 32768\n"
  "s_add_u32 s86, s66, 65536\n"
  "s_add_u32 s87, s66, 98304\n"
  "s_mov_b32 m0, 147456\n"
  "s_nop 0\n"
  "global_load_lds_dwordx4 v7, s[50:51] sc1\n"
  "global_load_lds_dwordx4 v7, s[50:51] offset:1024 sc1\n"
  "s_waitcnt lgkmcnt(1)\n"
  "v_smfmac_f32_16x16x64_bf16 v[32:35], v[88:91], v[128:135], v0\n"
  "v_smfmac_f32_16x16x64_bf16 v[36:39], v[88:91], v[144:151], v0\n"
  "s_waitcnt lgkmcnt(0)\n"
  "v_smfmac_f32_16x16x64_bf16 v[32:35], v[92:95], v[136:143], v0\n"
  "v_smfmac_f32_16x16x64_bf16 v[36:39], v[92:95], v[152:159], v0\n"
  "v_smfmac_f32_16x16x64_bf16 v[40:43], v[88:91], v[160:167], v0\n"
  "v_smfmac_f32_16x16x64_bf16 v[44:47], v[88:91], v[176:183], v0\n"
  "v_pk_add_f32 v[32:33], v[32:33], v[34:35]\n"
  "v_pk_add_f32 v[36:37], v[36:37], v[38:39]\n"
  "v_smfmac_f32_16x16x64_bf16 v[40:43], v[92:95], v[168:175], v0\n"
  "v_cvt_pk_bf16_f32 v32, v32, v33\n"
  "v_cvt_pk_bf16_f32 v33, v36, v37\n"
  "v_smfmac_f32_16x16x64_bf16 v[44:47], v[92:95], v[184:191], v0\n"
  "v_permlane16_swap_b32 v32, v33\n"
  "buffer_store_dwordx2 v[32:33], v8, s[56:59], s84 offen sc1\n"
  "v_smfmac_f32_16x16x64_bf16 v[48:51], v[88:91], v[192:199], v0\n"
  "v_smfmac_f32_16x16x64_bf16 v[52:55], v[88:91], v[208:215], v0\n"
  "v_pk_add_f32 v[40:41], v[40:41], v[42:43]\n"
  "v_pk_add_f32 v[44:45], v[44:45], v[46:47]\n"
  "v_smfmac_f32_16x16x64_bf16 v[48:51], v[92:95], v[200:207], v0\n"
  "v_cvt_pk_bf16_f32 v40, v40, v41\n"
  "v_cvt_pk_bf16_f32 v41, v44, v45\n"
  "v_smfmac_f32_16x16x64_bf16 v[52:55], v[92:95], v[216:223], v0\n"
  "v_permlane16_swap_b32 v40, v41\n"
  "buffer_store_dwordx2 v[40:41], v8, s[56:59], s85 offen sc1\n"
  "v_smfmac_f32_16x16x64_bf16 v[56:59], v[88:91], v[224:231], v0\n"
  "v_smfmac_f32_16x16x64_bf16 v[60:63], v[88:91], v[240:247], v0\n"
  "v_pk_add_f32 v[48:49], v[48:49], v[50:51]\n"
  "v_pk_add_f32 v[52:53], v[52:53], v[54:55]\n"
  "v_smfmac_f32_16x16x64_bf16 v[56:59], v[92:95], v[232:239], v0\n"
  "v_cvt_pk_bf16_f32 v48, v48, v49\n"
  "v_cvt_pk_bf16_f32 v49, v52, v53\n"
  "v_smfmac_f32_16x16x64_bf16 v[60:63], v[92:95], v[248:255], v0\n"
  "v_permlane16_swap_b32 v48, v49\n"
  "buffer_store_dwordx2 v[48:49], v8, s[56:59], s86 offen sc1\n"
  "v_pk_add_f32 v[56:57], v[56:57], v[58:59]\n"
  "s_nop 2\n"
  "v_pk_add_f32 v[60:61], v[60:61], v[62:63]\n"
  "v_cvt_pk_bf16_f32 v56, v56, v57\n"
  "v_cvt_pk_bf16_f32 v57, v60, v61\n"
  "s_nop 1\n"
  "v_permlane16_swap_b32 v56, v57\n"
  "buffer_store_dwordx2 v[56:57], v8, s[56:59], s87 offen sc1\n"
  "s_add_u32 s89, s88, 4096\n"
  "s_add_u32 s62, s62, 1\n"
  "s_add_u32 s63, s63, 4\n"
  "s_add_u32 s64, s64, 262144\n"
  "s_add_u32 s65, s65, 262144\n"
  "s_add_u32 s66, s66, 0x80000\n"
  "s_and_b32 s66, s66, 0x1fffff\n"
  "s_add_u32 s90, s90, 0x80000\n"
  "s_and_b32 s90, s90, 0x1fffff\n"
  "s_cmp_lt_u32 s62, 63\n"
  "s_cselect_b32 s74, 524288, 0\n"
  "s_add_u32 s50, s50, s74\n"
  "s_addc_u32 s51, s51, 0\n"
  "s_add_u32 s60, s60, s74\n"
  "s_addc_u32 s61, s61, 0\n"
  "s_mov_b32 s73, 200000\n"
  "s_sleep 5\n"
  "s_nop 11\n"
  "CH_p1_0_%=:\n"
  "buffer_load_dwordx4 v[32:35], v7, s[56:59], s88 offen sc1\n"
  "buffer_load_dwordx4 v[36:39], v7, s[56:59], s88 offen offset:1024 sc1\n"
  "buffer_load_dwordx4 v[40:43], v7, s[56:59], s88 offen offset:2048 sc1\n"
  "buffer_load_dwordx4 v[44:47], v7, s[56:59], s88 offen offset:3072 sc1\n"
  "buffer_load_dwordx4 v[48:51], v7, s[56:59], s89 offen sc1\n"
  "buffer_load_dwordx4 v[52:55], v7, s[56:59], s89 offen offset:1024 sc1\n"
  "buffer_load_dwordx4 v[56:59], v7, s[56:59], s89 offen offset:2048 sc1\n"
  "buffer_load_dwordx4 v[60:63], v7, s[56:59], s89 offen offset:3072 sc1\n"
  "s_sleep 6\n"
  "buffer_load_dwordx4 v[88:91], v7, s[56:59], s88 offen sc1\n"
  "buffer_load_dwordx4 v[92:95], v7, s[56:59], s88 offen offset:1024 sc1\n"
  "buffer_load_dwordx4 v[96:99], v7, s[56:59], s88 offen offset:2048 sc1\n"
  "buffer_load_dwordx4 v[100:103], v7, s[56:59], s88 offen offset:3072 sc1\n"
  "buffer_load_dwordx4 v[104:107], v7, s[56:59], s89 offen sc1\n"
  "buffer_load_dwordx4 v[108:111], v7, s[56:59], s89 offen offset:1024 sc1\n"
  "buffer_load_dwordx4 v[112:115], v7, s[56:59], s89 offen offset:2048 sc1\n"
  "buffer_load_dwordx4 v[116:119], v7, s[56:59], s89 offen offset:3072 sc1\n"
  "CH_p1a_0_%=:\n"
  "s_waitcnt vmcnt(15)\n"
  "v_mfma_f32_16x16x32_bf16 v[120:123], v[32:35], v[12:15], 0\n"
  "v_max_u32 v126, v32, v34\n"
  "s_waitcnt vmcnt(14)\n"
  "v_mfma_f32_16x16x32_bf16 v[120:123], v[36:39], v[12:15], v[120:123]\n"
  "v_max3_u32 v126, v126, v36, v38\n"
  "s_waitcnt vmcnt(13)\n"
  "v_mfma_f32_16x16x32_bf16 v[120:123], v[40:43], v[12:15], v[120:123]\n"
  "v_max3_u32 v126, v126, v40, v42\n"
  "s_waitcnt vmcnt(12)\n"
  "v_mfma_f32_16x16x32_bf16 v[120:123], v[44:47], v[12:15], v[120:123]\n"
  "v_max3_u32 v126, v126, v44, v46\n"
  "s_waitcnt vmcnt(11)\n"
  "v_mfma_f32_16x16x32_bf16 v[120:123], v[48:51], v[12:15], v[120:123]\n"
  "v_max3_u32 v126, v126, v48, v50\n"
  "s_waitcnt vmcnt(10)\n"
  "v_mfma_f32_16x16x32_bf16 v[120:123], v[52:55], v[12:15], v[120:123]\n"
  "v_max3_u32 v126, v126, v52, v54\n"
  "s_waitcnt vmcnt(9)\n"
  "v_mfma_f32_16x16x32_bf16 v[120:123], v[56:59], v[12:15], v[120:123]\n"
  "v_max3_u32 v126, v126, v56, v58\n"
  "s_waitcnt vmcnt(8)\n"
  "v_mfma_f32_16x16x32_bf16 v[120:123], v[60:63], v[12:15], v[120:123]\n"
  "v_max3_u32 v126, v126, v60, v62\n"
  "v_cmp_eq_u32 vcc, -1, v126\n"
  "s_cbranch_vccnz CH_p1retry_0_%=\n"
  "s_nop 3\n"
  "CH_p1ok_0_%=:\n"
  "v_cvt_pk_bf16_f32 v124, v120, v121\n"
  "v_cvt_pk_bf16_f32 v125, v122, v123\n"
  "s_mov_b64 exec, s[92:93]\n"
  "buffer_store_dwordx2 v[124:125], v9, s[56:59], s91 offen sc1\n"
  "s_mov_b64 exec, -1\n"
  "s_add_u32 s88, s88, 0x80000\n"
  "s_and_b32 s88, s88, 0x1fffff\n"
  "s_waitcnt vmcnt(1)\n"
  "ds_read_b128 v[88:91], v2\n"
  "ds_read_b128 v[92:95], v2 offset:1024\n"
  "ds_read_b128 v[96:99], v2 offset:2048\n"
  "ds_read_b128 v[100:103], v2 offset:3072\n"
  "ds_read_b128 v[104:107], v2 offset:4096\n"
  "ds_read_b128 v[108:111], v2 offset:5120\n"
  "ds_read_b128 v[112:115], v2 offset:6144\n"
  "ds_read_b128 v[116:119], v2 offset:7168\n"
  "v_mov_b64 v[64:65], 0\n"
  "v_mov_b64 v[66:67], 0\n"
  "v_mov_b64 v[68:69], 0\n"
  "v_mov_b64 v[70:71], 0\n"
  "v_mov_b64 v[72:73], 0\n"
  "v_mov_b64 v[74:75], 0\n"
  "v_mov_b64 v[76:77], 0\n"
  "v_mov_b64 v[78:79], 0\n"
  "v_mov_b64 v[80:81], 0\n"
  "v_mov_b64 v[82:83], 0\n"
  "v_mov_b64 v[84:85], 0\n"
  "v_mov_b64 v[86:87], 0\n"
  "s_waitcnt lgkmcnt(0)\n"
  "s_barrier\n"
  "s_sleep 4\n"
  "s_nop 3\n"
  "s_mov_b32 s73, 200000\n"
  "s_add_u32 s91, s91, 16384\n"
  "s_branch CH_p2_0_%=\n"
  "CH_p2retry_0_%=:\n"
  "s_waitcnt vmcnt(0)\n"
  "v_max3_u32 v48, v120, v122, v124\n"
  "v_max_u32 v48, v48, v126\n"
  "v_cmp_eq_u32 vcc, -1, v48\n"
  "s_cbranch_vccnz CH_p2again_0_%=\n"
  "v_mov_b64 v[32:33], v[120:121]\n"
  "v_mov_b64 v[34:35], v[122:123]\n"
  "v_mov_b64 v[36:37], v[124:125]\n"
  "v_mov_b64 v[38:39], v[126:127]\n"
  "s_branch CH_p2ok_0_%=\n"
  "CH_p2again_0_%=:\n"
  "s_sub_u32 s73, s73, 1\n"
  "s_cbranch_scc0 CH_p2_0_%=\n"
  "s_branch CH_END_%=\n"
  "CH_p1retry_0_%=:\n"
  "buffer_load_dwordx4 v[32:35], v7, s[56:59], s88 offen sc1\n"
  "buffer_load_dwordx4 v[36:39], v7, s[56:59], s88 offen offset:1024 sc1\n"
  "buffer_load_dwordx4 v[40:43], v7, s[56:59], s88 offen offset:2048 sc1\n"
  "buffer_load_dwordx4 v[44:47], v7, s[56:59], s88 offen offset:3072 sc1\n"
  "buffer_load_dwordx4 v[48:51], v7, s[56:59], s89 offen sc1\n"
  "buffer_load_dwordx4 v[52:55], v7, s[56:59], s89 offen offset:1024 sc1\n"
  "buffer_load_dwordx4 v[56:59], v7, s[56:59], s89 offen offset:2048 sc1\n"
  "buffer_load_dwordx4 v[60:63], v7, s[56:59], s89 offen offset:3072 sc1\n"
  "s_waitcnt vmcnt(15)\n"
  "v_mfma_f32_16x16x32_bf16 v[120:123], v[88:91], v[12:15], 0\n"
  "v_max_u32 v126, v88, v90\n"
  "s_waitcnt vmcnt(14)\n"
  "v_mfma_f32_16x16x32_bf16 v[120:123], v[92:95], v[12:15], v[120:123]\n"
  "v_max3_u32 v126, v126, v92, v94\n"
  "s_waitcnt vmcnt(13)\n"
  "v_mfma_f32_16x16x32_bf16 v[120:123], v[96:99], v[12:15], v[120:123]\n"
  "v_max3_u32 v126, v126, v96, v98\n"
  "s_waitcnt vmcnt(12)\n"
  "v_mfma_f32_16x16x32_bf16 v[120:123], v[100:103], v[12:15], v[120:123]\n"
  "v_max3_u32 v126, v126, v100, v102\n"
  "s_waitcnt vmcnt(11)\n"
  "v_mfma_f32_16x16x32_bf16 v[120:123], v[104:107], v[12:15], v[120:123]\n"
  "v_max3_u32 v126, v126, v104, v106\n"
  "s_waitcnt vmcnt(10)\n"
  "v_mfma_f32_16x16x32_bf16 v[120:123], v[108:111], v[12:15], v[120:123]\n"
  "v_max3_u32 v126, v126, v108, v110\n"
  "s_waitcnt vmcnt(9)\n"
  "v_mfma_f32_16x16x32_bf16 v[120:123], v[112:115], v[12:15], v[120:123]\n"
  "v_max3_u32 v126, v126, v112, v114\n"
  "s_waitcnt vmcnt(8)\n"
  "v_mfma_f32_16x16x32_bf16 v[120:123], v[116:119], v[12:15], v[120:123]\n"
  "v_max3_u32 v126, v126, v116, v118\n"
  "v_cmp_eq_u32 vcc, -1, v126\n"
  "s_cbranch_vccnz CH_p1again_0_%=\n"
  "s_nop 3\n"
  "s_branch CH_p1ok_0_%=\n"
  "CH_p1again_0_%=:\n"
  "buffer_load_dwordx4 v[88:91], v7, s[56:59], s88 offen sc1\n"
  "buffer_load_dwordx4 v[92:95], v7, s[56:59], s88 offen offset:1024 sc1\n"
  "buffer_load_dwordx4 v[96:99], v7, s[56:59], s88 offen offset:2048 sc1\n"
  "buffer_load_dwordx4 v[100:103], v7, s[56:59], s88 offen offset:3072 sc1\n"
  "buffer_load_dwordx4 v[104:107], v7, s[56:59], s89 offen sc1\n"
  "buffer_load_dwordx4 v[108:111], v7, s[56:59], s89 offen offset:1024 sc1\n"
  "buffer_load_dwordx4 v[112:115], v7, s[56:59], s89 offen offset:2048 sc1\n"
  "buffer_load_dwordx4 v[116:119], v7, s[56:59], s89 offen offset:3072 sc1\n"
  "s_sub_u32 s73, s73, 1\n"
  "s_cbranch_scc0 CH_p1a_0_%=\n"
  "s_branch CH_END_%=\n"
  "CH_exit_0_%=:\n"
  "s_waitcnt vmcnt(0)\n"
  "s_barrier\n"
  "s_branch CH_END_%=\n"
  "CH_start_1_%=:\n"
  "s_add_u32 s60, s60, 2048\n"
  "s_addc_u32 s61, s61, 0\n"
  "s_mov_b32 s64, 0\n"
  "s_mov_b32 s65, 3072\n"
  "s_mov_b32 s62, 0\n"
  "s_lshl_b32 s74, s69, 8\n"
  "s_add_u32 s66, s74, 131072\n"
  "s_lshl_b32 s88, s69, 14\n"
  "s_add_u32 s90, s88, 1572864\n"
  "s_add_u32 s67, s99, 18432\n"
  "s_add_u32 s91, s74, s99\n"
  "s_add_u32 s91, s91, 16384\n"
  "v_add_u32 v22, 2304, v4\n"
  "s_add_u32 s50, s60, 8192\n"
  "s_addc_u32 s51, s61, 0\n"
  "s_mov_b32 m0, 141312\n"
  "s_nop 0\n"
  "global_load_lds_dwordx4 v7, s[60:61] sc1\n"
  "global_load_lds_dwordx4 v7, s[60:61] offset:1024 sc1\n"
  "s_mov_b32 m0, 149504\n"
  "s_nop 0\n"
  "global_load_lds_dwordx4 v7, s[50:51] sc1\n"
  "global_load_lds_dwordx4 v7, s[50:51] offset:1024 sc1\n"
  "s_mov_b32 s74, 524288\n"
  "s_add_u32 s50, s50, s74\n"
  "s_addc_u32 s51, s51, 0\n"
  "s_add_u32 s60, s60, s74\n"
  "s_addc_u32 s61, s61, 0\n"
  "global_load_dwordx4 v[40:43], v112, s[80:81] offset:256\n"
  "global_load_dwordx4 v[44:47], v112, s[80:81] offset:384\n"
  "ds_read_b128 v[88:91], v2 offset:8192\n"
  "ds_read_b128 v[92:95], v2 offset:9216\n"
  "ds_read_b128 v[96:99], v2 offset:10240\n"
  "ds_read_b128 v[100:103], v2 offset:11264\n"
  "ds_read_b128 v[104:107], v2 offset:12288\n"
  "ds_read_b128 v[108:111], v2 offset:13312\n"
  "ds_read_b128 v[112:115], v2 offset:14336\n"
  "ds_read_b128 v[116:119], v2 offset:15360\n"
  "v_mov_b64 v[64:65], 0\n"
  "v_mov_b64 v[66:67], 0\n"
  "v_mov_b64 v[68:69], 0\n"
  "v_mov_b64 v[70:71], 0\n"
  "v_mov_b64 v[72:73], 0\n"
  "v_mov_b64 v[74:75], 0\n"
  "v_mov_b64 v[76:77], 0\n"
  "v_mov_b64 v[78:79], 0\n"
  "v_mov_b64 v[80:81], 0\n"
  "v_mov_b64 v[82:83], 0\n"
  "v_mov_b64 v[84:85], 0\n"
  "v_mov_b64 v[86:87], 0\n"
  "s_waitcnt vmcnt(0) lgkmcnt(0)\n"
  "s_barrier\n"
  "s_branch CH_loop_1_%=\n"
  "CH_p2_1_%=:\n"
  "buffer_load_dwordx4 v[40:43], v7, s[56:59], s67 offen sc1\n"
  "buffer_load_dwordx4 v[44:47], v7, s[56:59], s67 offen offset:1024 sc1\n"
  "s_sleep 5\n"
  "buffer_load_dwordx4 v[120:123], v7, s[56:59], s67 offen sc1\n"
  "buffer_load_dwordx4 v[124:127], v7, s[56:59], s67 offen offset:1024 sc1\n"
  "s_waitcnt vmcnt(2)\n"
  "v_max3_u32 v56, v40, v42, v44\n"
  "v_max_u32 v56, v56, v46\n"
  "v_cmp_eq_u32 vcc, -1, v56\n"
  "s_cbranch_vccnz CH_p2retry_1_%=\n"
  "CH_p2ok_1_%=:\n"
  "s_add_u32 s67, s67, 16384\n"
  "CH_loop_1_%=:\n"
  "ds_write_b128 v1, v[40:43] offset:2048\n"
  "ds_write_b128 v1, v[44:47] offset:3072\n"
  "v_smfmac_f32_16x16x64_bf16 v[64:67], v[40:43], a[64:71], v0\n"
  "v_smfmac_f32_16x16x64_bf16 v[68:71], v[40:43], a[72:79], v0\n"
  "v_smfmac_f32_16x16x64_bf16 v[64:67], v[44:47], a[96:103], v0\n"
  "v_smfmac_f32_16x16x64_bf16 v[68:71], v[44:47], a[104:111], v0\n"
  "s_waitcnt lgkmcnt(0)\n"
  "s_barrier\n"
  "ds_read_b128 v[48:51], v1 offset:4096\n"
  "ds_read_b128 v[52:55], v1 offset:5120\n"
  "ds_read_b128 v[56:59], v1 offset:6144\n"
  "ds_read_b128 v[60:63], v1 offset:7168\n"
  "v_smfmac_f32_16x16x64_bf16 v[80:83], v[40:43], v[88:95], v0\n"
  "ds_read_b128 v[88:91], v3\n"
  "ds_read_b128 v[92:95], v3 offset:1024\n"
  "v_smfmac_f32_16x16x64_bf16 v[84:87], v[40:43], v[96:103], v0\n"
  "ds_read_b128 v[96:99], v3 offset:2048\n"
  "ds_read_b128 v[100:103], v3 offset:3072\n"
  "v_smfmac_f32_16x16x64_bf16 v[80:83], v[44:47], v[104:111], v0\n"
  "ds_read_b128 v[104:107], v3 offset:4096\n"
  "ds_read_b128 v[108:111], v3 offset:5120\n"
  "v_smfmac_f32_16x16x64_bf16 v[84:87], v[44:47], v[112:119], v0\n"
  "ds_read_b128 v[112:115], v3 offset:6144\n"
  "ds_read_b128 v[116:119], v3 offset:7168\n"
  "s_waitcnt vmcnt(0)\n"
  "ds_read_b128 v[32:35], v1\n"
  "ds_read_b128 v[36:39], v1 offset:1024\n"
  "v_smfmac_f32_16x16x64_bf16 v[72:75], v[40:43], a[80:87], v0\n"
  "v_smfmac_f32_16x16x64_bf16 v[76:79], v[40:43], a[88:95], v0\n"
  "v_smfmac_f32_16x16x64_bf16 v[72:75], v[44:47], a[112:119], v0\n"
  "v_smfmac_f32_16x16x64_bf16 v[76:79], v[44:47], a[120:127], v0\n"
  "s_waitcnt lgkmcnt(10)\n"
  "v_smfmac_f32_16x16x64_bf16 v[64:67], v[48:51], a[128:135], v0\n"
  "v_smfmac_f32_16x16x64_bf16 v[68:71], v[48:51], a[136:143], v0\n"
  "v_smfmac_f32_16x16x64_bf16 v[64:67], v[52:55], a[160:167], v0\n"
  "v_smfmac_f32_16x16x64_bf16 v[68:71], v[52:55], a[168:175], v0\n"
  "ds_read_b128 v[40:43], v1 offset:8192\n"
  "v_smfmac_f32_16x16x64_bf16 v[64:67], v[56:59], a[192:199], v0\n"
  "v_smfmac_f32_16x16x64_bf16 v[68:71], v[56:59], a[200:207], v0\n"
  "ds_read_b128 v[44:47], v1 offset:12288\n"
  "v_smfmac_f32_16x16x64_bf16 v[64:67], v[60:63], a[224:231], v0\n"
  "v_smfmac_f32_16x16x64_bf16 v[68:71], v[60:63], a[232:239], v0\n"
  "ds_read_b128 v[120:123], v1 offset:9216\n"
  "s_waitcnt lgkmcnt(3)\n"
  "v_smfmac_f32_16x16x64_bf16 v[64:67], v[32:35], a[0:7], v0\n"
  "v_smfmac_f32_16x16x64_bf16 v[68:71], v[32:35], a[8:15], v0\n"
  "ds_read_b128 v[124:127], v1 offset:13312\n"
  "v_smfmac_f32_16x16x64_bf16 v[64:67], v[36:39], a[32:39], v0\n"
  "v_smfmac_f32_16x16x64_bf16 v[68:71], v[36:39], a[40:47], v0\n"
  "v_smfmac_f32_16x16x64_bf16 v[72:75], v[48:51], a[144:151], v0\n"
  "v_smfmac_f32_16x16x64_bf16 v[76:79], v[48:51], a[152:159], v0\n"
  "v_pk_add_f32 v[64:65], v[64:65], v[66:67]\n"
  "v_pk_add_f32 v[68:69], v[68:69], v[70:71]\n"
  "v_smfmac_f32_16x16x64_bf16 v[80:83], v[48:51], v[88:95], v0\n"
  "ds_read_b128 v[88:91], v3 offset:8192\n"
  "ds_read_b128 v[92:95], v3 offset:9216\n"
  "v_cvt_pk_bf16_f32 v64, v64, v68\n"
  "v_cvt_pk_bf16_f32 v65, v65, v69\n"
  "v_smfmac_f32_16x16x64_bf16 v[84:87], v[48:51], v[96:103], v0\n"
  "ds_read_b128 v[96:99], v3 offset:10240\n"
  "ds_read_b128 v[100:103], v3 offset:11264\n"
  "ds_write2_b32 v4, v64, v65 offset0:0 offset1:72\n"
  "v_smfmac_f32_16x16x64_bf16 v[72:75], v[52:55], a[176:183], v0\n"
  "v_smfmac_f32_16x16x64_bf16 v[76:79], v[52:55], a[184:191], v0\n"
  "v_smfmac_f32_16x16x64_bf16 v[80:83], v[52:55], v[104:111], v0\n"
  "ds_read_b128 v[104:107], v3 offset:12288\n"
  "ds_read_b128 v[108:111], v3 offset:13312\n"
  "v_smfmac_f32_16x16x64_bf16 v[84:87], v[52:55], v[112:119], v0\n"
  "ds_read_b128 v[112:115], v3 offset:14336\n"
  "ds_read_b128 v[116:119], v3 offset:15360\n"
  "v_smfmac_f32_16x16x64_bf16 v[72:75], v[56:59], a[208:215], v0\n"
  "s_waitcnt lgkmcnt(4)\n"
  "s_barrier\n"
  "ds_read_b128 v[64:67], v5\n"
  "ds_read_b128 v[68:71], v5 offset:64\n"
  "ds_read_b128 v[48:51], v5 offset:128\n"
  "ds_read_b128 v[52:55], v5 offset:192\n"
  "v_smfmac_f32_16x16x64_bf16 v[76:79], v[56:59], a[216:223], v0\n"
  "v_smfmac_f32_16x16x64_bf16 v[80:83], v[56:59], v[88:95], v0\n"
  "ds_read_b128 v[88:91], v2\n"
  "ds_read_b128 v[92:95], v2 offset:1024\n"
  "v_smfmac_f32_16x16x64_bf16 v[84:87], v[56:59], v[96:103], v0\n"
  "ds_read_b128 v[96:99], v2 offset:2048\n"
  "ds_read_b128 v[100:103], v2 offset:3072\n"
  "ds_read_b128 v[56:59], v1 offset:10240\n"
  "v_smfmac_f32_16x16x64_bf16 v[72:75], v[60:63], a[240:247], v0\n"
  "v_smfmac_f32_16x16x64_bf16 v[76:79], v[60:63], a[248:255], v0\n"
  "s_waitcnt lgkmcnt(11)\n"
  "v_smfmac_f32_16x16x64_bf16 v[80:83], v[60:63], v[104:111], v0\n"
  "s_waitcnt lgkmcnt(9)\n"
  "v_smfmac_f32_16x16x64_bf16 v[84:87], v[60:63], v[112:119], v0\n"
  "ds_read_b128 v[60:63], v1 offset:14336\n"
  "ds_read_b128 v[104:107], v1 offset:11264\n"
  "ds_read_b128 v[112:115], v1 offset:15360\n"
  "v_smfmac_f32_16x16x64_bf16 v[72:75], v[32:35], a[16:23], v0\n"
  "v_smfmac_f32_16x16x64_bf16 v[76:79], v[32:35], a[24:31], v0\n"
  "v_smfmac_f32_16x16x64_bf16 v[72:75], v[36:39], a[48:55], v0\n"
  "v_smfmac_f32_16x16x64_bf16 v[76:79], v[36:39], a[56:63], v0\n"
  "s_waitcnt lgkmcnt(6)\n"
  "v_mfma_f32_16x16x32_bf16 v[40:43], v[40:43], v[64:67], 0\n"
  "v_mfma_f32_16x16x32_bf16 v[44:47], v[44:47], v[64:67], 0\n"
  "v_mfma_f32_16x16x32_bf16 v[40:43], v[120:123], v[68:71], v[40:43]\n"
  "v_pk_add_f32 v[72:73], v[72:73], v[74:75]\n"
  "v_pk_add_f32 v[76:77], v[76:77], v[78:79]\n"
  "v_mfma_f32_16x16x32_bf16 v[44:47], v[124:127], v[68:71], v[44:47]\n"
  "v_cvt_pk_bf16_f32 v72, v72, v76\n"
  "v_cvt_pk_bf16_f32 v73, v73, v77\n"
  "s_waitcnt lgkmcnt(3)\n"
  "v_mfma_f32_16x16x32_bf16 v[40:43], v[56:59], v[48:51], v[40:43]\n"
  "ds_write2_b32 v22, v72, v73 offset0:0 offset1:72\n"
  "s_waitcnt lgkmcnt(3)\n"
  "v_mfma_f32_16x16x32_bf16 v[44:47], v[60:63], v[48:51], v[44:47]\n"
  "s_waitcnt lgkmcnt(2)\n"
  "v_mfma_f32_16x16x32_bf16 v[40:43], v[104:107], v[52:55], v[40:43]\n"
  "s_waitcnt lgkmcnt(1)\n"
  "v_mfma_f32_16x16x32_bf16 v[44:47], v[112:115], v[52:55], v[44:47]\n"
  "v_smfmac_f32_16x16x64_bf16 v[80:83], v[32:35], v[88:95], v0\n"
  "ds_read_b128 v[88:91], v2 offset:4096\n"
  "ds_read_b128 v[92:95], v2 offset:5120\n"
  "v_smfmac_f32_16x16x64_bf16 v[84:87], v[32:35], v[96:103], v0\n"
  "ds_read_b128 v[96:99], v2 offset:6144\n"
  "ds_read_b128 v[100:103], v2 offset:7168\n"
  "s_waitcnt lgkmcnt(4)\n"
  "s_barrier\n"
  "ds_read_b128 v[72:75], v5 offset:2304\n"
  "ds_read_b128 v[76:79], v5 offset:2368\n"
  "ds_read_b128 v[120:123], v5 offset:2432\n"
  "ds_read_b128 v[124:127], v5 offset:2496\n"
  "ds_read_b128 v[56:59], v1 offset:18432\n"
  "ds_read_b128 v[60:63], v1 offset:19456\n"
  "s_mov_b32 m0, 141312\n"
  "s_nop 0\n"
  "global_load_lds_dwordx4 v7, s[60:61] sc1\n"
  "global_load_lds_dwordx4 v7, s[60:61] offset:1024 sc1\n"
  "v_max3_f32 v104, v40, v41, v42\n"
  "v_max3_f32 v105, v43, v44, v45\n"
  "v_max3_f32 v104, v104, v46, v47\n"
  "v_max_f32 v104, v104, v105\n"
  "s_waitcnt lgkmcnt(8)\n"
  "v_smfmac_f32_16x16x64_bf16 v[80:83], v[36:39], v[88:95], v0\n"
  "s_waitcnt lgkmcnt(6)\n"
  "v_smfmac_f32_16x16x64_bf16 v[84:87], v[36:39], v[96:103], v0\n"
  "s_waitcnt lgkmcnt(5)\n"
  "v_mfma_f32_16x16x32_bf16 v[112:115], v[72:75], v[64:67], 0\n"
  "s_waitcnt lgkmcnt(4)\n"
  "v_mfma_f32_16x16x32_bf16 v[112:115], v[76:79], v[68:71], v[112:115]\n"
  "s_waitcnt lgkmcnt(3)\n"
  "v_mfma_f32_16x16x32_bf16 v[112:115], v[120:123], v[48:51], v[112:115]\n"
  "s_waitcnt lgkmcnt(2)\n"
  "v_mfma_f32_16x16x32_bf16 v[112:115], v[124:127], v[52:55], v[112:115]\n"
  "s_mov_b64 exec, s[70:71]\n"
  "buffer_store_dwordx4 v[76:79], v10, s[52:55], s64 offen offset:512 sc1\n"
  "s_mov_b64 exec, -1\n"
  "s_nop 3\n"
  "v_max3_f32 v104, v104, v112, v113\n"
  "v_mov_b32 v105, v104\n"
  "s_nop 1\n"
  "v_permlane32_swap_b32 v104, v105\n"
  "v_max_f32 v104, v104, v105\n"
  "v_mov_b32 v105, v104\n"
  "s_nop 1\n"
  "v_permlane16_swap_b32 v104, v105\n"
  "v_max_f32 v104, v104, v105\n"
  "v_mul_f32 v104, s72, v104\n"
  "v_pk_fma_f32 v[40:41], v[40:41], s[72:73], v[104:105] op_sel_hi:[1,0,0] neg_lo:[0,0,1] neg_hi:[0,0,1]\n"
  "v_pk_fma_f32 v[42:43], v[42:43], s[72:73], v[104:105] op_sel_hi:[1,0,0] neg_lo:[0,0,1] neg_hi:[0,0,1]\n"
  "v_pk_fma_f32 v[44:45], v[44:45], s[72:73], v[104:105] op_sel_hi:[1,0,0] neg_lo:[0,0,1] neg_hi:[0,0,1]\n"
  "v_pk_fma_f32 v[46:47], v[46:47], s[72:73], v[104:105] op_sel_hi:[1,0,0] neg_lo:[0,0,1] neg_hi:[0,0,1]\n"
  "v_exp_f32 v40, v40\n"
  "v_exp_f32 v41, v41\n"
  "v_exp_f32 v42, v42\n"
  "v_exp_f32 v43, v43\n"
  "v_exp_f32 v44, v44\n"
  "v_exp_f32 v45, v45\n"
  "v_exp_f32 v46, v46\n"
  "v_exp_f32 v47, v47\n"
  "v_cvt_pk_bf16_f32 v48, v40, v41\n"
  "v_cvt_pk_bf16_f32 v49, v42, v43\n"
  "v_cvt_pk_bf16_f32 v50, v44, v45\n"
  "v_cvt_pk_bf16_f32 v51, v46, v47\n"
  "v_pk_fma_f32 v[112:113], v[112:113], s[72:73], v[104:105] op_sel_hi:[1,0,0] neg_lo:[0,0,1] neg_hi:[0,0,1]\n"
  "s_nop 0\n"
  "v_mfma_f32_16x16x32_bf16 v[52:55], v[18:21], v[48:51], 0\n"
  "v_exp_f32 v112, v112\n"
  "v_exp_f32 v113, v113\n"
  "s_waitcnt lgkmcnt(0)\n"
  "v_mfma_f32_16x16x32_bf16 v[64:67], v[56:59], v[48:51], 0\n"
  "v_cvt_pk_bf16_f32 v24, v80, v81\n"
  "v_cvt_pk_bf16_f32 v25, v82, v83\n"
  "v_mfma_f32_16x16x32_bf16 v[68:71], v[60:63], v[48:51], 0\n"
  "v_cvt_pk_bf16_f32 v28, v112, v113\n"
  "v_cvt_pk_bf16_f32 v29, v112, v113\n"
  "v_cvt_pk_bf16_f32 v26, v84, v85\n"
  "v_cvt_pk_bf16_f32 v27, v86, v87\n"
  "v_mfma_f32_16x16x32_bf16 v[52:55], v[16:19], v[28:31], v[52:55]\n"
  "v_mfma_f32_16x16x32_bf16 v[64:67], v[24:27], v[28:31], v[64:67]\n"
  "v_mfma_f32_16x16x32_bf16 v[68:71], v[26:29], v[28:31], v[68:71]\n"
  "v_mov_b64 v[32:33], 0\n"
  "v_mov_b64 v[34:35], 0\n"
  "v_mov_b64 v[36:37], 0\n"
  "v_mov_b64 v[38:39], 0\n"
  "v_rcp_f32 v106, v52\n"
  "v_mov_b64 v[40:41], 0\n"
  "v_pk_mul_f32 v[64:65], v[64:65], v[106:107] op_sel_hi:[1,0]\n"
  "v_pk_mul_f32 v[66:67], v[66:67], v[106:107] op_sel_hi:[1,0]\n"
  "v_pk_mul_f32 v[68:69], v[68:69], v[106:107] op_sel_hi:[1,0]\n"
  "v_pk_mul_f32 v[70:71], v[70:71], v[106:107] op_sel_hi:[1,0]\n"
  "v_cvt_pk_bf16_f32 v84, v64, v65\n"
  "v_cvt_pk_bf16_f32 v85, v66, v67\n"
  "v_cvt_pk_bf16_f32 v86, v68, v69\n"
  "v_cvt_pk_bf16_f32 v87, v70, v71\n"
  "ds_write_b128 v6, v[84:87]\n"
  "buffer_store_dwordx2 v[24:25], v11, s[52:55], s65 offen sc1\n"
  "buffer_store_dwordx2 v[26:27], v11, s[52:55], s65 offen offset:512 sc1\n"
  "s_cmp_eq_u32 s62, 63\n"
  "s_cbranch_scc1 CH_exit_1_%=\n"
  "v_mov_b64 v[42:43], 0\n"
  "v_mov_b64 v[44:45], 0\n"
  "v_mov_b64 v[46:47], 0\n"
  "s_waitcnt lgkmcnt(0)\n"
  "s_barrier\n"
  "ds_read_b128 v[88:91], v1 offset:29184\n"
  "ds_read_b128 v[92:95], v1 offset:30208\n"
  "v_mov_b64 v[48:49], 0\n"
  "v_mov_b64 v[50:51], 0\n"
  "v_mov_b64 v[52:53], 0\n"
  "v_mov_b64 v[54:55], 0\n"
  "v_mov_b64 v[56:57], 0\n"
  "v_mov_b64 v[58:59], 0\n"
  "v_mov_b64 v[60:61], 0\n"
  "v_mov_b64 v[62:63], 0\n"
  "s_add_u32 s84, s66, 0\n"
  "s_add_u32 s85, s66, 32768\n"
  "s_add_u32 s86, s66, 65536\n"
  "s_add_u32 s87, s66, 98304\n"
  "s_mov_b32 m0, 149504\n"
  "s_nop 0\n"
  "global_load_lds_dwordx4 v7, s[50:51] sc1\n"
  "global_load_lds_dwordx4 v7, s[50:51] offset:1024 sc1\n"
  "s_waitcnt lgkmcnt(1)\n"
  "v_smfmac_f32_16x16x64_bf16 v[32:35], v[88:91], v[128:135], v0\n"
  "v_smfmac_f32_16x16x64_bf16 v[36:39], v[88:91], v[144:151], v0\n"
  "s_waitcnt lgkmcnt(0)\n"
  "v_smfmac_f32_16x16x64_bf16 v[32:35], v[92:95], v[136:143], v0\n"
  "v_smfmac_f32_16x16x64_bf16 v[36:39], v[92:95], v[152:159], v0\n"
  "v_smfmac_f32_16x16x64_bf16 v[40:43], v[88:91], v[160:167], v0\n"
  "v_smfmac_f32_16x16x64_bf16 v[44:47], v[88:91], v[176:183], v0\n"
  "v_pk_add_f32 v[32:33], v[32:33], v[34:35]\n"
  "v_pk_add_f32 v[36:37], v[36:37], v[38:39]\n"
  "v_smfmac_f32_16x16x64_bf16 v[40:43], v[92:95], v[168:175], v0\n"
  "v_cvt_pk_bf16_f32 v32, v32, v33\n"
  "v_cvt_pk_bf16_f32 v33, v36, v37\n"
  "v_smfmac_f32_16x16x64_bf16 v[44:47], v[92:95], v[184:191], v0\n"
  "v_permlane16_swap_b32 v32, v33\n"
  "buffer_store_dwordx2 v[32:33], v8, s[56:59], s84 offen sc1\n"
  "v_smfmac_f32_16x16x64_bf16 v[48:51], v[88:91], v[192:199], v0\n"
  "v_smfmac_f32_16x16x64_bf16 v[52:55], v[88:91], v[208:215], v0\n"
  "v_pk_add_f32 v[40:41], v[40:41], v[42:43]\n"
  "v_pk_add_f32 v[44:45], v[44:45], v[46:47]\n"
  "v_smfmac_f32_16x16x64_bf16 v[48:51], v[92:95], v[200:207], v0\n"
  "v_cvt_pk_bf16_f32 v40, v40, v41\n"
  "v_cvt_pk_bf16_f32 v41, v44, v45\n"
  "v_smfmac_f32_16x16x64_bf16 v[52:55], v[92:95], v[216:223], v0\n"
  "v_permlane16_swap_b32 v40, v41\n"
  "buffer_store_dwordx2 v[40:41], v8, s[56:59], s85 offen sc1\n"
  "v_smfmac_f32_16x16x64_bf16 v[56:59], v[88:91], v[224:231], v0\n"
  "v_smfmac_f32_16x16x64_bf16 v[60:63], v[88:91], v[240:247], v0\n"
  "v_pk_add_f32 v[48:49], v[48:49], v[50:51]\n"
  "v_pk_add_f32 v[52:53], v[52:53], v[54:55]\n"
  "v_smfmac_f32_16x16x64_bf16 v[56:59], v[92:95], v[232:239], v0\n"
  "v_cvt_pk_bf16_f32 v48, v48, v49\n"
  "v_cvt_pk_bf16_f32 v49, v52, v53\n"
  "v_smfmac_f32_16x16x64_bf16 v[60:63], v[92:95], v[248:255], v0\n"
  "v_permlane16_swap_b32 v48, v49\n"
  "buffer_store_dwordx2 v[48:49], v8, s[56:59], s86 offen sc1\n"
  "v_pk_add_f32 v[56:57], v[56:57], v[58:59]\n"
  "s_nop 2\n"
  "v_pk_add_f32 v[60:61], v[60:61], v[62:63]\n"
  "v_cvt_pk_bf16_f32 v56, v56, v57\n"
  "v_cvt_pk_bf16_f32 v57, v60, v61\n"
  "s_nop 1\n"
  "v_permlane16_swap_b32 v56, v57\n"
  "buffer_store_dwordx2 v[56:57], v8, s[56:59], s87 offen sc1\n"
  "v_mov_b32 v36, -1\n"
  "v_mov_b32 v37, -1\n"
  "v_mov_b32 v38, -1\n"
  "v_mov_b32 v39, -1\n"
  "buffer_store_dwordx4 v[36:39], v7, s[56:59], s90 offen sc1\n"
  "buffer_store_dwordx4 v[36:39], v7, s[56:59], s90 offen offset:1024 sc1\n"
  "buffer_store_dwordx4 v[36:39], v7, s[56:59], s90 offen offset:2048 sc1\n"
  "s_add_u32 s74, s62, 2\n"
  "s_min_u32 s74, s74, 63\n"
  "s_nop 0\n"
  "s_cmp_gt_u32 s78, s74\n"
  "s_cbranch_scc1 CH_kvw_1_%=_ok\n"
  "s_mov_b32 s73, 200000\n"
  "v_lshrrev_b32 v32, 2, v7\n"
  "CH_kvw_1_%=_p:\n"
  "global_load_dword v33, v32, s[76:77] sc1\n"
  "s_waitcnt vmcnt(0)\n"
  "v_cmp_eq_u32 vcc, -1, v33\n"
  "s_ff1_i32_b64 s78, vcc\n"
  "s_cmp_lt_i32 s78, 0\n"
  "s_cselect_b32 s78, 64, s78\n"
  "s_cmp_gt_u32 s78, s74\n"
  "s_cbranch_scc1 CH_kvw_1_%=_ok\n"
  "s_sleep 2\n"
  "s_sub_u32 s73, s73, 1\n"
  "s_cbranch_scc0 CH_kvw_1_%=_p\n"
  "s_branch CH_END_%=\n"
  "CH_kvw_1_%=_ok:\n"
  "ds_read_b128 v[88:91], v2 offset:8192\n"
  "ds_read_b128 v[92:95], v2 offset:9216\n"
  "ds_read_b128 v[96:99], v2 offset:10240\n"
  "ds_read_b128 v[100:103], v2 offset:11264\n"
  "ds_read_b128 v[104:107], v2 offset:12288\n"
  "ds_read_b128 v[108:111], v2 offset:13312\n"
  "ds_read_b128 v[112:115], v2 offset:14336\n"
  "ds_read_b128 v[116:119], v2 offset:15360\n"
  "v_mov_b64 v[64:65], 0\n"
  "v_mov_b64 v[66:67], 0\n"
  "v_mov_b64 v[68:69], 0\n"
  "v_mov_b64 v[70:71], 0\n"
  "v_mov_b64 v[72:73], 0\n"
  "v_mov_b64 v[74:75], 0\n"
  "v_mov_b64 v[76:77], 0\n"
  "v_mov_b64 v[78:79], 0\n"
  "v_mov_b64 v[80:81], 0\n"
  "v_mov_b64 v[82:83], 0\n"
  "v_mov_b64 v[84:85], 0\n"
  "v_mov_b64 v[86:87], 0\n"
  "s_add_u32 s62, s62, 1\n"
  "s_add_u32 s63, s63, 4\n"
  "s_add_u32 s64, s64, 262144\n"
  "s_add_u32 s65, s65, 262144\n"
  "s_add_u32 s66, s66, 0x80000\n"
  "s_and_b32 s66, s66, 0x1fffff\n"
  "s_add_u32 s90, s90, 0x80000\n"
  "s_and_b32 s90, s90, 0x1fffff\n"
  "s_cmp_lt_u32 s62, 63\n"
  "s_cselect_b32 s74, 524288, 0\n"
  "s_add_u32 s50, s50, s74\n"
  "s_addc_u32 s51, s51, 0\n"
  "s_add_u32 s60, s60, s74\n"
  "s_addc_u32 s61, s61, 0\n"
  "s_waitcnt lgkmcnt(0)\n"
  "s_barrier\n"
  "s_sleep 4\n"
  "s_nop 3\n"
  "s_mov_b32 s73, 200000\n"
  "s_add_u32 s91, s91, 16384\n"
  "s_branch CH_p2_1_%=\n"
  "CH_p2retry_1_%=:\n"
  "s_waitcnt vmcnt(0)\n"
  "v_max3_u32 v56, v120, v122, v124\n"
  "v_max_u32 v56, v56, v126\n"
  "v_cmp_eq_u32 vcc, -1, v56\n"
  "s_cbranch_vccnz CH_p2again_1_%=\n"
  "v_mov_b64 v[40:41], v[120:121]\n"
  "v_mov_b64 v[42:43], v[122:123]\n"
  "v_mov_b64 v[44:45], v[124:125]\n"
  "v_mov_b64 v[46:47], v[126:127]\n"
  "s_branch CH_p2ok_1_%=\n"
  "CH_p2again_1_%=:\n"
  "s_sub_u32 s73, s73, 1\n"
  "s_cbranch_scc0 CH_p2_1_%=\n"
  "s_branch CH_END_%=\n"
  "CH_exit_1_%=:\n"
  "s_waitcnt vmcnt(0)\n"
  "s_barrier\n"
  "s_branch CH_END_%=\n"
  "CH_start_2_%=:\n"
  "s_add_u32 s60, s60, 4096\n"
  "s_addc_u32 s61, s61, 0\n"
  "s_mov_b32 s64, 0\n"
  "s_mov_b32 s65, 4096\n"
  "s_mov_b32 s62, 0\n"
  "s_lshl_b32 s74, s69, 8\n"
  "s_add_u32 s66, s74, 262144\n"
  "s_lshl_b32 s88, s69, 14\n"
  "s_add_u32 s90, s88, 1575936\n"
  "s_add_u32 s67, s99, 20480\n"
  "s_add_u32 s91, s74, s99\n"
  "s_add_u32 s91, s91, 16384\n"
  "v_add_u32 v22, 2304, v4\n"
  "s_add_u32 s50, s60, 8192\n"
  "s_addc_u32 s51, s61, 0\n"
  "s_mov_b32 m0, 143360\n"
  "s_nop 0\n"
  "global_load_lds_dwordx4 v7, s[60:61] sc1\n"
  "global_load_lds_dwordx4 v7, s[60:61] offset:1024 sc1\n"
  "s_mov_b32 m0, 151552\n"
  "s_nop 0\n"
  "global_load_lds_dwordx4 v7, s[50:51] sc1\n"
  "global_load_lds_dwordx4 v7, s[50:51] offset:1024 sc1\n"
  "s_mov_b32 s74, 524288\n"
  "s_add_u32 s50, s50, s74\n"
  "s_addc_u32 s51, s51, 0\n"
  "s_add_u32 s60, s60, s74\n"
  "s_addc_u32 s61, s61, 0\n"
  "global_load_dwordx4 v[48:51], v112, s[80:81] offset:512\n"
  "global_load_dwordx4 v[52:55], v112, s[80:81] offset:640\n"
  "ds_read_b128 v[88:91], v3\n"
  "ds_read_b128 v[92:95], v3 offset:1024\n"
  "ds_read_b128 v[96:99], v3 offset:2048\n"
  "ds_read_b128 v[100:103], v3 offset:3072\n"
  "ds_read_b128 v[104:107], v3 offset:4096\n"
  "ds_read_b128 v[108:111], v3 offset:5120\n"
  "ds_read_b128 v[112:115], v3 offset:6144\n"
  "ds_read_b128 v[116:119], v3 offset:7168\n"
  "v_mov_b64 v[64:65], 0\n"
  "v_mov_b64 v[66:67], 0\n"
  "v_mov_b64 v[68:69], 0\n"
  "v_mov_b64 v[70:71], 0\n"
  "v_mov_b64 v[72:73], 0\n"
  "v_mov_b64 v[74:75], 0\n"
  "v_mov_b64 v[76:77], 0\n"
  "v_mov_b64 v[78:79], 0\n"
  "v_mov_b64 v[80:81], 0\n"
  "v_mov_b64 v[82:83], 0\n"
  "v_mov_b64 v[84:85], 0\n"
  "v_mov_b64 v[86:87], 0\n"
  "s_waitcnt vmcnt(0) lgkmcnt(0)\n"
  "s_barrier\n"
  "s_branch CH_loop_2_%=\n"
  "CH_p2_2_%=:\n"
  "buffer_load_dwordx4 v[48:51], v7, s[56:59], s67 offen sc1\n"
  "buffer_load_dwordx4 v[52:55], v7, s[56:59], s67 offen offset:1024 sc1\n"
  "s_sleep 5\n"
  "buffer_load_dwordx4 v[120:123], v7, s[56:59], s67 offen sc1\n"
  "buffer_load_dwordx4 v[124:127], v7, s[56:59], s67 offen offset:1024 sc1\n"
  "s_waitcnt vmcnt(2)\n"
  "v_max3_u32 v32, v48, v50, v52\n"
  "v_max_u32 v32, v32, v54\n"
  "v_cmp_eq_u32 vcc, -1, v32\n"
  "s_cbranch_vccnz CH_p2retry_2_%=\n"
  "CH_p2ok_2_%=:\n"
  "s_add_u32 s67, s67, 16384\n"
  "CH_loop_2_%=:\n"
  "ds_write_b128 v1, v[48:51] offset:4096\n"
  "ds_write_b128 v1, v[52:55] offset:5120\n"
  "v_smfmac_f32_16x16x64_bf16 v[64:67], v[48:51], a[128:135], v0\n"
  "v_smfmac_f32_16x16x64_bf16 v[68:71], v[48:51], a[136:143], v0\n"
  "v_smfmac_f32_16x16x64_bf16 v[64:67], v[52:55], a[160:167], v0\n"
  "v_smfmac_f32_16x16x64_bf16 v[68:71], v[52:55], a[168:175], v0\n"
  "s_waitcnt lgkmcnt(0)\n"
  "s_barrier\n"
  "ds_read_b128 v[56:59], v1 offset:6144\n"
  "ds_read_b128 v[60:63], v1 offset:7168\n"
  "ds_read_b128 v[32:35], v1\n"
  "ds_read_b128 v[36:39], v1 offset:1024\n"
  "v_smfmac_f32_16x16x64_bf16 v[80:83], v[48:51], v[88:95], v0\n"
  "ds_read_b128 v[88:91], v3 offset:8192\n"
  "ds_read_b128 v[92:95], v3 offset:9216\n"
  "v_smfmac_f32_16x16x64_bf16 v[84:87], v[48:51], v[96:103], v0\n"
  "ds_read_b128 v[96:99], v3 offset:10240\n"
  "ds_read_b128 v[100:103], v3 offset:11264\n"
  "v_smfmac_f32_16x16x64_bf16 v[80:83], v[52:55], v[104:111], v0\n"
  "ds_read_b128 v[104:107], v3 offset:12288\n"
  "ds_read_b128 v[108:111], v3 offset:13312\n"
  "v_smfmac_f32_16x16x64_bf16 v[84:87], v[52:55], v[112:119], v0\n"
  "ds_read_b128 v[112:115], v3 offset:14336\n"
  "ds_read_b128 v[116:119], v3 offset:15360\n"
  "s_waitcnt vmcnt(0)\n"
  "ds_read_b128 v[40:43], v1 offset:2048\n"
  "ds_read_b128 v[44:47], v1 offset:3072\n"
  "v_smfmac_f32_16x16x64_bf16 v[72:75], v[48:51], a[144:151], v0\n"
  "v_smfmac_f32_16x16x64_bf16 v[76:79], v[48:51], a[152:159], v0\n"
  "v_smfmac_f32_16x16x64_bf16 v[72:75], v[52:55], a[176:183], v0\n"
  "v_smfmac_f32_16x16x64_bf16 v[76:79], v[52:55], a[184:191], v0\n"
  "s_waitcnt lgkmcnt(10)\n"
  "v_smfmac_f32_16x16x64_bf16 v[64:67], v[56:59], a[192:199], v0\n"
  "v_smfmac_f32_16x16x64_bf16 v[68:71], v[56:59], a[200:207], v0\n"
  "v_smfmac_f32_16x16x64_bf16 v[64:67], v[60:63], a[224:231], v0\n"
  "v_smfmac_f32_16x16x64_bf16 v[68:71], v[60:63], a[232:239], v0\n"
  "ds_read_b128 v[48:51], v1 offset:8192\n"
  "v_smfmac_f32_16x16x64_bf16 v[64:67], v[32:35], a[0:7], v0\n"
  "v_smfmac_f32_16x16x64_bf16 v[68:71], v[32:35], a[8:15], v0\n"
  "ds_read_b128 v[52:55], v1 offset:12288\n"
  "v_smfmac_f32_16x16x64_bf16 v[64:67], v[36:39], a[32:39], v0\n"
  "v_smfmac_f32_16x16x64_bf16 v[68:71], v[36:39], a[40:47], v0\n"
  "ds_read_b128 v[120:123], v1 offset:9216\n"
  "s_waitcnt lgkmcnt(3)\n"
  "v_smfmac_f32_16x16x64_bf16 v[64:67], v[40:43], a[64:71], v0\n"
  "v_smfmac_f32_16x16x64_bf16 v[68:71], v[40:43], a[72:79], v0\n"
  "ds_read_b128 v[124:127], v1 offset:13312\n"
  "v_smfmac_f32_16x16x64_bf16 v[64:67], v[44:47], a[96:103], v0\n"
  "v_smfmac_f32_16x16x64_bf16 v[68:71], v[44:47], a[104:111], v0\n"
  "v_smfmac_f32_16x16x64_bf16 v[72:75], v[56:59], a[208:215], v0\n"
  "v_smfmac_f32_16x16x64_bf16 v[76:79], v[56:59], a[216:223], v0\n"
  "v_pk_add_f32 v[64:65], v[64:65], v[66:67]\n"
  "v_pk_add_f32 v[68:69], v[68:69], v[70:71]\n"
  "v_smfmac_f32_16x16x64_bf16 v[80:83], v[56:59], v[88:95], v0\n"
  "ds_read_b128 v[88:91], v2\n"
  "ds_read_b128 v[92:95], v2 offset:1024\n"
  "v_cvt_pk_bf16_f32 v64, v64, v68\n"
  "v_cvt_pk_bf16_f32 v65, v65, v69\n"
  "v_smfmac_f32_16x16x64_bf16 v[84:87], v[56:59], v[96:103], v0\n"
  "ds_read_b128 v[96:99], v2 offset:2048\n"
  "ds_read_b128 v[100:103], v2 offset:3072\n"
  "ds_write2_b32 v4, v64, v65 offset0:0 offset1:72\n"
  "v_smfmac_f32_16x16x64_bf16 v[72:75], v[60:63], a[240:247], v0\n"
  "v_smfmac_f32_16x16x64_bf16 v[76:79], v[60:63], a[248:255], v0\n"
  "v_smfmac_f32_16x16x64_bf16 v[80:83], v[60:63], v[104:111], v0\n"
  "ds_read_b128 v[104:107], v2 offset:4096\n"
  "ds_read_b128 v[108:111], v2 offset:5120\n"
  "v_smfmac_f32_16x16x64_bf16 v[84:87], v[60:63], v[112:119], v0\n"
  "ds_read_b128 v[112:115], v2 offset:6144\n"
  "ds_read_b128 v[116:119], v2 offset:7168\n"
  "v_smfmac_f32_16x16x64_bf16 v[72:75], v[32:35], a[16:23], v0\n"
  "s_waitcnt lgkmcnt(4)\n"
  "s_barrier\n"
  "ds_read_b128 v[64:67], v5\n"
  "ds_read_b128 v[68:71], v5 offset:64\n"
  "ds_read_b128 v[56:59], v5 offset:128\n"
  "ds_read_b128 v[60:63], v5 offset:192\n"
  "v_smfmac_f32_16x16x64_bf16 v[76:79], v[32:35], a[24:31], v0\n"
  "v_smfmac_f32_16x16x64_bf16 v[80:83], v[32:35], v[88:95], v0\n"
  "ds_read_b128 v[88:91], v2 offset:8192\n"
  "ds_read_b128 v[92:95], v2 offset:9216\n"
  "v_smfmac_f32_16x16x64_bf16 v[84:87], v[32:35], v[96:103], v0\n"
  "ds_read_b128 v[96:99], v2 offset:10240\n"
  "ds_read_b128 v[100:103], v2 offset:11264\n"
  "ds_read_b128 v[32:35], v1 offset:10240\n"
  "v_smfmac_f32_16x16x64_bf16 v[72:75], v[36:39], a[48:55], v0\n"
  "v_smfmac_f32_16x16x64_bf16 v[76:79], v[36:39], a[56:63], v0\n"
  "s_waitcnt lgkmcnt(11)\n"
  "v_smfmac_f32_16x16x64_bf16 v[80:83], v[36:39], v[104:111], v0\n"
  "s_waitcnt lgkmcnt(9)\n"
  "v_smfmac_f32_16x16x64_bf16 v[84:87], v[36:39], v[112:119], v0\n"
  "ds_read_b128 v[36:39], v1 offset:14336\n"
  "ds_read_b128 v[104:107], v1 offset:11264\n"
  "ds_read_b128 v[112:115], v1 offset:15360\n"
  "v_smfmac_f32_16x16x64_bf16 v[72:75], v[40:43], a[80:87], v0\n"
  "v_smfmac_f32_16x16x64_bf16 v[76:79], v[40:43], a[88:95], v0\n"
  "v_smfmac_f32_16x16x64_bf16 v[72:75], v[44:47], a[112:119], v0\n"
  "v_smfmac_f32_16x16x64_bf16 v[76:79], v[44:47], a[120:127], v0\n"
  "s_waitcnt lgkmcnt(6)\n"
  "v_mfma_f32_16x16x32_bf16 v[48:51], v[48:51], v[64:67], 0\n"
  "v_mfma_f32_16x16x32_bf16 v[52:55], v[52:55], v[64:67], 0\n"
  "v_mfma_f32_16x16x32_bf16 v[48:51], v[120:123], v[68:71], v[48:51]\n"
  "v_pk_add_f32 v[72:73], v[72:73], v[74:75]\n"
  "v_pk_add_f32 v[76:77], v[76:77], v[78:79]\n"
  "v_mfma_f32_16x16x32_bf16 v[52:55], v[124:127], v[68:71], v[52:55]\n"
  "v_cvt_pk_bf16_f32 v72, v72, v76\n"
  "v_cvt_pk_bf16_f32 v73, v73, v77\n"
  "s_waitcnt lgkmcnt(3)\n"
  "v_mfma_f32_16x16x32_bf16 v[48:51], v[32:35], v[56:59], v[48:51]\n"
  "ds_write2_b32 v22, v72, v73 offset0:0 offset1:72\n"
  "s_waitcnt lgkmcnt(3)\n"
  "v_mfma_f32_16x16x32_bf16 v[52:55], v[36:39], v[56:59], v[52:55]\n"
  "s_waitcnt lgkmcnt(2)\n"
  "v_mfma_f32_16x16x32_bf16 v[48:51], v[104:107], v[60:63], v[48:51]\n"
  "s_waitcnt lgkmcnt(1)\n"
  "v_mfma_f32_16x16x32_bf16 v[52:55], v[112:115], v[60:63], v[52:55]\n"
  "v_smfmac_f32_16x16x64_bf16 v[80:83], v[40:43], v[88:95], v0\n"
  "ds_read_b128 v[88:91], v2 offset:12288\n"
  "ds_read_b128 v[92:95], v2 offset:13312\n"
  "v_smfmac_f32_16x16x64_bf16 v[84:87], v[40:43], v[96:103], v0\n"
  "ds_read_b128 v[96:99], v2 offset:14336\n"
  "ds_read_b128 v[100:103], v2 offset:15360\n"
  "s_waitcnt lgkmcnt(4)\n"
  "s_barrier\n"
  "ds_read_b128 v[72:75], v5 offset:2304\n"
  "ds_read_b128 v[76:79], v5 offset:2368\n"
  "ds_read_b128 v[120:123], v5 offset:2432\n"
  "ds_read_b128 v[124:127], v5 offset:2496\n"
  "ds_read_b128 v[32:35], v1 offset:20480\n"
  "ds_read_b128 v[36:39], v1 offset:21504\n"
  "s_mov_b32 m0, 143360\n"
  "s_nop 0\n"
  "global_load_lds_dwordx4 v7, s[60:61] sc1\n"
  "global_load_lds_dwordx4 v7, s[60:61] offset:1024 sc1\n"
  "v_max3_f32 v104, v48, v49, v50\n"
  "v_max3_f32 v105, v51, v52, v53\n"
  "v_max3_f32 v104, v104, v54, v55\n"
  "v_max_f32 v104, v104, v105\n"
  "s_waitcnt lgkmcnt(8)\n"
  "v_smfmac_f32_16x16x64_bf16 v[80:83], v[44:47], v[88:95], v0\n"
  "s_waitcnt lgkmcnt(6)\n"
  "v_smfmac_f32_16x16x64_bf16 v[84:87], v[44:47], v[96:103], v0\n"
  "s_waitcnt lgkmcnt(5)\n"
  "v_mfma_f32_16x16x32_bf16 v[112:115], v[72:75], v[64:67], 0\n"
  "s_waitcnt lgkmcnt(4)\n"
  "v_mfma_f32_16x16x32_bf16 v[112:115], v[76:79], v[68:71], v[112:115]\n"
  "s_waitcnt lgkmcnt(3)\n"
  "v_mfma_f32_16x16x32_bf16 v[112:115], v[120:123], v[56:59], v[112:115]\n"
  "s_waitcnt lgkmcnt(2)\n"
  "v_mfma_f32_16x16x32_bf16 v[112:115], v[124:127], v[60:63], v[112:115]\n"
  "s_mov_b64 exec, s[70:71]\n"
  "buffer_store_dwordx4 v[120:123], v10, s[52:55], s64 offen offset:1024 sc1\n"
  "s_mov_b64 exec, -1\n"
  "s_nop 3\n"
  "v_max3_f32 v104, v104, v112, v113\n"
  "v_mov_b32 v105, v104\n"
  "s_nop 1\n"
  "v_permlane32_swap_b32 v104, v105\n"
  "v_max_f32 v104, v104, v105\n"
  "v_mov_b32 v105, v104\n"
  "s_nop 1\n"
  "v_permlane16_swap_b32 v104, v105\n"
  "v_max_f32 v104, v104, v105\n"
  "v_mul_f32 v104, s72, v104\n"
  "v_pk_fma_f32 v[48:49], v[48:49], s[72:73], v[104:105] op_sel_hi:[1,0,0] neg_lo:[0,0,1] neg_hi:[0,0,1]\n"
  "v_pk_fma_f32 v[50:51], v[50:51], s[72:73], v[104:105] op_sel_hi:[1,0,0] neg_lo:[0,0,1] neg_hi:[0,0,1]\n"
  "v_pk_fma_f32 v[52:53], v[52:53], s[72:73], v[104:105] op_sel_hi:[1,0,0] neg_lo:[0,0,1] neg_hi:[0,0,1]\n"
  "v_pk_fma_f32 v[54:55], v[54:55], s[72:73], v[104:105] op_sel_hi:[1,0,0] neg_lo:[0,0,1] neg_hi:[0,0,1]\n"
  "v_exp_f32 v48, v48\n"
  "v_exp_f32 v49, v49\n"
  "v_exp_f32 v50, v50\n"
  "v_exp_f32 v51, v51\n"
  "v_exp_f32 v52, v52\n"
  "v_exp_f32 v53, v53\n"
  "v_exp_f32 v54, v54\n"
  "v_exp_f32 v55, v55\n"
  "v_cvt_pk_bf16_f32 v56, v48, v49\n"
  "v_cvt_pk_bf16_f32 v57, v50, v51\n"
  "v_cvt_pk_bf16_f32 v58, v52, v53\n"
  "v_cvt_pk_bf16_f32 v59, v54, v55\n"
  "v_pk_fma_f32 v[112:113], v[112:113], s[72:73], v[104:105] op_sel_hi:[1,0,0] neg_lo:[0,0,1] neg_hi:[0,0,1]\n"
  "s_nop 0\n"
  "v_mfma_f32_16x16x32_bf16 v[60:63], v[18:21], v[56:59], 0\n"
  "v_exp_f32 v112, v112\n"
  "v_exp_f32 v113, v113\n"
  "s_waitcnt lgkmcnt(0)\n"
  "v_mfma_f32_16x16x32_bf16 v[64:67], v[32:35], v[56:59], 0\n"
  "v_cvt_pk_bf16_f32 v24, v80, v81\n"
  "v_cvt_pk_bf16_f32 v25, v82, v83\n"
  "v_mfma_f32_16x16x32_bf16 v[68:71], v[36:39], v[56:59], 0\n"
  "v_cvt_pk_bf16_f32 v28, v112, v113\n"
  "v_cvt_pk_bf16_f32 v29, v112, v113\n"
  "v_cvt_pk_bf16_f32 v26, v84, v85\n"
  "v_cvt_pk_bf16_f32 v27, v86, v87\n"
  "v_mfma_f32_16x16x32_bf16 v[60:63], v[16:19], v[28:31], v[60:63]\n"
  "v_mfma_f32_16x16x32_bf16 v[64:67], v[24:27], v[28:31], v[64:67]\n"
  "v_mfma_f32_16x16x32_bf16 v[68:71], v[26:29], v[28:31], v[68:71]\n"
  "v_mov_b64 v[40:41], 0\n"
  "v_mov_b64 v[42:43], 0\n"
  "v_mov_b64 v[44:45], 0\n"
  "v_mov_b64 v[46:47], 0\n"
  "v_rcp_f32 v106, v60\n"
  "v_mov_b64 v[48:49], 0\n"
  "v_pk_mul_f32 v[64:65], v[64:65], v[106:107] op_sel_hi:[1,0]\n"
  "v_pk_mul_f32 v[66:67], v[66:67], v[106:107] op_sel_hi:[1,0]\n"
  "v_pk_mul_f32 v[68:69], v[68:69], v[106:107] op_sel_hi:[1,0]\n"
  "v_pk_mul_f32 v[70:71], v[70:71], v[106:107] op_sel_hi:[1,0]\n"
  "v_cvt_pk_bf16_f32 v84, v64, v65\n"
  "v_cvt_pk_bf16_f32 v85, v66, v67\n"
  "v_cvt_pk_bf16_f32 v86, v68, v69\n"
  "v_cvt_pk_bf16_f32 v87, v70, v71\n"
  "ds_write_b128 v6, v[84:87]\n"
  "buffer_store_dwordx2 v[24:25], v11, s[52:55], s65 offen sc1\n"
  "buffer_store_dwordx2 v[26:27], v11, s[52:55], s65 offen offset:512 sc1\n"
  "s_cmp_eq_u32 s62, 63\n"
  "s_cbranch_scc1 CH_exit_2_%=\n"
  "v_mov_b64 v[50:51], 0\n"
  "v_mov_b64 v[52:53], 0\n"
  "v_mov_b64 v[54:55], 0\n"
  "s_waitcnt lgkmcnt(0)\n"
  "s_barrier\n"
  "ds_read_b128 v[88:91], v1 offset:29184\n"
  "ds_read_b128 v[92:95], v1 offset:30208\n"
  "v_mov_b64 v[32:33], 0\n"
  "v_mov_b64 v[34:35], 0\n"
  "v_mov_b64 v[36:37], 0\n"
  "v_mov_b64 v[38:39], 0\n"
  "v_mov_b64 v[56:57], 0\n"
  "v_mov_b64 v[58:59], 0\n"
  "v_mov_b64 v[60:61], 0\n"
  "v_mov_b64 v[62:63], 0\n"
  "s_add_u32 s84, s66, 0\n"
  "s_add_u32 s85, s66, 32768\n"
  "s_add_u32 s86, s66, 65536\n"
  "s_add_u32 s87, s66, 98304\n"
  "s_mov_b32 m0, 151552\n"
  "s_nop 0\n"
  "global_load_lds_dwordx4 v7, s[50:51] sc1\n"
  "global_load_lds_dwordx4 v7, s[50:51] offset:1024 sc1\n"
  "s_waitcnt lgkmcnt(1)\n"
  "v_smfmac_f32_16x16x64_bf16 v[32:35], v[88:91], v[128:135], v0\n"
  "v_smfmac_f32_16x16x64_bf16 v[36:39], v[88:91], v[144:151], v0\n"
  "s_waitcnt lgkmcnt(0)\n"
  "v_smfmac_f32_16x16x64_bf16 v[32:35], v[92:95], v[136:143], v0\n"
  "v_smfmac_f32_16x16x64_bf16 v[36:39], v[92:95], v[152:159], v0\n"
  "v_smfmac_f32_16x16x64_bf16 v[40:43], v[88:91], v[160:167], v0\n"
  "v_smfmac_f32_16x16x64_bf16 v[44:47], v[88:91], v[176:183], v0\n"
  "v_pk_add_f32 v[32:33], v[32:33], v[34:35]\n"
  "v_pk_add_f32 v[36:37], v[36:37], v[38:39]\n"
  "v_smfmac_f32_16x16x64_bf16 v[40:43], v[92:95], v[168:175], v0\n"
  "v_cvt_pk_bf16_f32 v32, v32, v33\n"
  "v_cvt_pk_bf16_f32 v33, v36, v37\n"
  "v_smfmac_f32_16x16x64_bf16 v[44:47], v[92:95], v[184:191], v0\n"
  "v_permlane16_swap_b32 v32, v33\n"
  "buffer_store_dwordx2 v[32:33], v8, s[56:59], s84 offen sc1\n"
  "v_smfmac_f32_16x16x64_bf16 v[48:51], v[88:91], v[192:199], v0\n"
  "v_smfmac_f32_16x16x64_bf16 v[52:55], v[88:91], v[208:215], v0\n"
  "v_pk_add_f32 v[40:41], v[40:41], v[42:43]\n"
  "v_pk_add_f32 v[44:45], v[44:45], v[46:47]\n"
  "v_smfmac_f32_16x16x64_bf16 v[48:51], v[92:95], v[200:207], v0\n"
  "v_cvt_pk_bf16_f32 v40, v40, v41\n"
  "v_cvt_pk_bf16_f32 v41, v44, v45\n"
  "v_smfmac_f32_16x16x64_bf16 v[52:55], v[92:95], v[216:223], v0\n"
  "v_permlane16_swap_b32 v40, v41\n"
  "buffer_store_dwordx2 v[40:41], v8, s[56:59], s85 offen sc1\n"
  "v_smfmac_f32_16x16x64_bf16 v[56:59], v[88:91], v[224:231], v0\n"
  "v_smfmac_f32_16x16x64_bf16 v[60:63], v[88:91], v[240:247], v0\n"
  "v_pk_add_f32 v[48:49], v[48:49], v[50:51]\n"
  "v_pk_add_f32 v[52:53], v[52:53], v[54:55]\n"
  "v_smfmac_f32_16x16x64_bf16 v[56:59], v[92:95], v[232:239], v0\n"
  "v_cvt_pk_bf16_f32 v48, v48, v49\n"
  "v_cvt_pk_bf16_f32 v49, v52, v53\n"
  "v_smfmac_f32_16x16x64_bf16 v[60:63], v[92:95], v[248:255], v0\n"
  "v_permlane16_swap_b32 v48, v49\n"
  "buffer_store_dwordx2 v[48:49], v8, s[56:59], s86 offen sc1\n"
  "v_pk_add_f32 v[56:57], v[56:57], v[58:59]\n"
  "s_nop 2\n"
  "v_pk_add_f32 v[60:61], v[60:61], v[62:63]\n"
  "v_cvt_pk_bf16_f32 v56, v56, v57\n"
  "v_cvt_pk_bf16_f32 v57, v60, v61\n"
  "s_nop 1\n"
  "v_permlane16_swap_b32 v56, v57\n"
  "buffer_store_dwordx2 v[56:57], v8, s[56:59], s87 offen sc1\n"
  "v_mov_b32 v36, -1\n"
  "v_mov_b32 v37, -1\n"
  "v_mov_b32 v38, -1\n"
  "v_mov_b32 v39, -1\n"
  "buffer_store_dwordx4 v[36:39], v7, s[56:59], s90 offen sc1\n"
  "buffer_store_dwordx4 v[36:39], v7, s[56:59], s90 offen offset:1024 sc1\n"
  "buffer_store_dwordx4 v[36:39], v7, s[56:59], s90 offen offset:2048 sc1\n"
  "ds_read_b128 v[88:91], v3\n"
  "ds_read_b128 v[92:95], v3 offset:1024\n"
  "ds_read_b128 v[96:99], v3 offset:2048\n"
  "ds_read_b128 v[100:103], v3 offset:3072\n"
  "ds_read_b128 v[104:107], v3 offset:4096\n"
  "ds_read_b128 v[108:111], v3 offset:5120\n"
  "ds_read_b128 v[112:115], v3 offset:6144\n"
  "ds_read_b128 v[116:119], v3 offset:7168\n"
  "v_mov_b64 v[64:65], 0\n"
  "v_mov_b64 v[66:67], 0\n"
  "v_mov_b64 v[68:69], 0\n"
  "v_mov_b64 v[70:71], 0\n"
  "v_mov_b64 v[72:73], 0\n"
  "v_mov_b64 v[74:75], 0\n"
  "v_mov_b64 v[76:77], 0\n"
  "v_mov_b64 v[78:79], 0\n"
  "v_mov_b64 v[80:81], 0\n"
  "v_mov_b64 v[82:83], 0\n"
  "v_mov_b64 v[84:85], 0\n"
  "v_mov_b64 v[86:87], 0\n"
  "s_add_u32 s62, s62, 1\n"
  "s_add_u32 s63, s63, 4\n"
  "s_add_u32 s64, s64, 262144\n"
  "s_add_u32 s65, s65, 262144\n"
  "s_add_u32 s66, s66, 0x80000\n"
  "s_and_b32 s66, s66, 0x1fffff\n"
  "s_add_u32 s90, s90, 0x80000\n"
  "s_and_b32 s90, s90, 0x1fffff\n"
  "s_cmp_lt_u32 s62, 63\n"
  "s_cselect_b32 s74, 524288, 0\n"
  "s_add_u32 s50, s50, s74\n"
  "s_addc_u32 s51, s51, 0\n"
  "s_add_u32 s60, s60, s74\n"
  "s_addc_u32 s61, s61, 0\n"
  "s_waitcnt lgkmcnt(0)\n"
  "s_barrier\n"
  "s_sleep 4\n"
  "s_nop 3\n"
  "s_mov_b32 s73, 200000\n"
  "s_add_u32 s91, s91, 16384\n"
  "s_branch CH_p2_2_%=\n"
  "CH_p2retry_2_%=:\n"
  "s_waitcnt vmcnt(0)\n"
  "v_max3_u32 v32, v120, v122, v124\n"
  "v_max_u32 v32, v32, v126\n"
  "v_cmp_eq_u32 vcc, -1, v32\n"
  "s_cbranch_vccnz CH_p2again_2_%=\n"
  "v_mov_b64 v[48:49], v[120:121]\n"
  "v_mov_b64 v[50:51], v[122:123]\n"
  "v_mov_b64 v[52:53], v[124:125]\n"
  "v_mov_b64 v[54:55], v[126:127]\n"
  "s_branch CH_p2ok_2_%=\n"
  "CH_p2again_2_%=:\n"
  "s_sub_u32 s73, s73, 1\n"
  "s_cbranch_scc0 CH_p2_2_%=\n"
  "s_branch CH_END_%=\n"
  "CH_exit_2_%=:\n"
  "s_waitcnt vmcnt(0)\n"
  "s_barrier\n"
  "s_branch CH_END_%=\n"
  "CH_start_3_%=:\n"
  "s_add_u32 s60, s60, 6144\n"
  "s_addc_u32 s61, s61, 0\n"
  "s_mov_b32 s64, 0\n"
  "s_mov_b32 s65, 5120\n"
  "s_mov_b32 s62, 0\n"
  "s_lshl_b32 s74, s69, 8\n"
  "s_add_u32 s66, s74, 393216\n"
  "s_lshl_b32 s88, s69, 14\n"
  "s_add_u32 s90, s88, 1579008\n"
  "s_add_u32 s67, s99, 22528\n"
  "s_add_u32 s91, s74, s99\n"
  "s_add_u32 s91, s91, 16384\n"
  "v_add_u32 v22, 2304, v4\n"
  "s_add_u32 s50, s60, 8192\n"
  "s_addc_u32 s51, s61, 0\n"
  "s_mov_b32 m0, 145408\n"
  "s_nop 0\n"
  "global_load_lds_dwordx4 v7, s[60:61] sc1\n"
  "global_load_lds_dwordx4 v7, s[60:61] offset:1024 sc1\n"
  "s_mov_b32 m0, 153600\n"
  "s_nop 0\n"
  "global_load_lds_dwordx4 v7, s[50:51] sc1\n"
  "global_load_lds_dwordx4 v7, s[50:51] offset:1024 sc1\n"
  "s_mov_b32 s74, 524288\n"
  "s_add_u32 s50, s50, s74\n"
  "s_addc_u32 s51, s51, 0\n"
  "s_add_u32 s60, s60, s74\n"
  "s_addc_u32 s61, s61, 0\n"
  "global_load_dwordx4 v[56:59], v112, s[80:81] offset:768\n"
  "global_load_dwordx4 v[60:63], v112, s[80:81] offset:896\n"
  "ds_read_b128 v[88:91], v3 offset:8192\n"
  "ds_read_b128 v[92:95], v3 offset:9216\n"
  "ds_read_b128 v[96:99], v3 offset:10240\n"
  "ds_read_b128 v[100:103], v3 offset:11264\n"
  "ds_read_b128 v[104:107], v3 offset:12288\n"
  "ds_read_b128 v[108:111], v3 offset:13312\n"
  "ds_read_b128 v[112:115], v3 offset:14336\n"
  "ds_read_b128 v[116:119], v3 offset:15360\n"
  "v_mov_b64 v[64:65], 0\n"
  "v_mov_b64 v[66:67], 0\n"
  "v_mov_b64 v[68:69], 0\n"
  "v_mov_b64 v[70:71], 0\n"
  "v_mov_b64 v[72:73], 0\n"
  "v_mov_b64 v[74:75], 0\n"
  "v_mov_b64 v[76:77], 0\n"
  "v_mov_b64 v[78:79], 0\n"
  "v_mov_b64 v[80:81], 0\n"
  "v_mov_b64 v[82:83], 0\n"
  "v_mov_b64 v[84:85], 0\n"
  "v_mov_b64 v[86:87], 0\n"
  "s_waitcnt vmcnt(0) lgkmcnt(0)\n"
  "s_barrier\n"
  "s_branch CH_loop_3_%=\n"
  "CH_p2_3_%=:\n"
  "buffer_load_dwordx4 v[56:59], v7, s[56:59], s67 offen sc1\n"
  "buffer_load_dwordx4 v[60:63], v7, s[56:59], s67 offen offset:1024 sc1\n"
  "s_sleep 5\n"
  "buffer_load_dwordx4 v[120:123], v7, s[56:59], s67 offen sc1\n"
  "buffer_load_dwordx4 v[124:127], v7, s[56:59], s67 offen offset:1024 sc1\n"
  "s_waitcnt vmcnt(2)\n"
  "v_max3_u32 v40, v56, v58, v60\n"
  "v_max_u32 v40, v40, v62\n"
  "v_cmp_eq_u32 vcc, -1, v40\n"
  "s_cbranch_vccnz CH_p2retry_3_%=\n"
  "CH_p2ok_3_%=:\n"
  "s_add_u32 s67, s67, 16384\n"
  "CH_loop_3_%=:\n"
  "ds_write_b128 v1, v[56:59] offset:6144\n"
  "ds_write_b128 v1, v[60:63] offset:7168\n"
  "v_smfmac_f32_16x16x64_bf16 v[64:67], v[56:59], a[192:199], v0\n"
  "v_smfmac_f32_16x16x64_bf16 v[68:71], v[56:59], a[200:207], v0\n"
  "v_smfmac_f32_16x16x64_bf16 v[64:67], v[60:63], a[224:231], v0\n"
  "v_smfmac_f32_16x16x64_bf16 v[68:71], v[60:63], a[232:239], v0\n"
  "s_waitcnt lgkmcnt(0)\n"
  "s_barrier\n"
  "ds_read_b128 v[32:35], v1\n"
  "ds_read_b128 v[36:39], v1 offset:1024\n"
  "ds_read_b128 v[40:43], v1 offset:2048\n"
  "ds_read_b128 v[44:47], v1 offset:3072\n"
  "v_smfmac_f32_16x16x64_bf16 v[80:83], v[56:59], v[88:95], v0\n"
  "ds_read_b128 v[88:91], v2\n"
  "ds_read_b128 v[92:95], v2 offset:1024\n"
  "v_smfmac_f32_16x16x64_bf16 v[84:87], v[56:59], v[96:103], v0\n"
  "ds_read_b128 v[96:99], v2 offset:2048\n"
  "ds_read_b128 v[100:103], v2 offset:3072\n"
  "v_smfmac_f32_16x16x64_bf16 v[80:83], v[60:63], v[104:111], v0\n"
  "ds_read_b128 v[104:107], v2 offset:4096\n"
  "ds_read_b128 v[108:111], v2 offset:5120\n"
  "v_smfmac_f32_16x16x64_bf16 v[84:87], v[60:63], v[112:119], v0\n"
  "ds_read_b128 v[112:115], v2 offset:6144\n"
  "ds_read_b128 v[116:119], v2 offset:7168\n"
  "s_waitcnt vmcnt(0)\n"
  "ds_read_b128 v[48:51], v1 offset:4096\n"
  "ds_read_b128 v[52:55], v1 offset:5120\n"
  "v_smfmac_f32_16x16x64_bf16 v[72:75], v[56:59], a[208:215], v0\n"
  "v_smfmac_f32_16x16x64_bf16 v[76:79], v[56:59], a[216:223], v0\n"
  "v_smfmac_f32_16x16x64_bf16 v[72:75], v[60:63], a[240:247], v0\n"
  "v_smfmac_f32_16x16x64_bf16 v[76:79], v[60:63], a[248:255], v0\n"
  "s_waitcnt lgkmcnt(10)\n"
  "v_smfmac_f32_16x16x64_bf16 v[64:67], v[32:35], a[0:7], v0\n"
  "v_smfmac_f32_16x16x64_bf16 v[68:71], v[32:35], a[8:15], v0\n"
  "v_smfmac_f32_16x16x64_bf16 v[64:67], v[36:39], a[32:39], v0\n"
  "v_smfmac_f32_16x16x64_bf16 v[68:71], v[36:39], a[40:47], v0\n"
  "ds_read_b128 v[56:59], v1 offset:8192\n"
  "v_smfmac_f32_16x16x64_bf16 v[64:67], v[40:43], a[64:71], v0\n"
  "v_smfmac_f32_16x16x64_bf16 v[68:71], v[40:43], a[72:79], v0\n"
  "ds_read_b128 v[60:63], v1 offset:12288\n"
  "v_smfmac_f32_16x16x64_bf16 v[64:67], v[44:47], a[96:103], v0\n"
  "v_smfmac_f32_16x16x64_bf16 v[68:71], v[44:47], a[104:111], v0\n"
  "s_cmp_eq_u32 s62, 0\n"
  "s_cbranch_scc1 CH_nofl_3_%=\n"
  "s_mov_b64 exec, 1\n"
  "buffer_store_dword v0, off, s[56:59], s63 sc1\n"
  "s_mov_b64 exec, -1\n"
  "CH_nofl_3_%=:\n"
  "ds_read_b128 v[120:123], v1 offset:9216\n"
  "s_waitcnt lgkmcnt(3)\n"
  "v_smfmac_f32_16x16x64_bf16 v[64:67], v[48:51], a[128:135], v0\n"
  "v_smfmac_f32_16x16x64_bf16 v[68:71], v[48:51], a[136:143], v0\n"
  "ds_read_b128 v[124:127], v1 offset:13312\n"
  "v_smfmac_f32_16x16x64_bf16 v[64:67], v[52:55], a[160:167], v0\n"
  "v_smfmac_f32_16x16x64_bf16 v[68:71], v[52:55], a[168:175], v0\n"
  "v_smfmac_f32_16x16x64_bf16 v[72:75], v[32:35], a[16:23], v0\n"
  "v_smfmac_f32_16x16x64_bf16 v[76:79], v[32:35], a[24:31], v0\n"
  "v_pk_add_f32 v[64:65], v[64:65], v[66:67]\n"
  "v_pk_add_f32 v[68:69], v[68:69], v[70:71]\n"
  "v_smfmac_f32_16x16x64_bf16 v[80:83], v[32:35], v[88:95], v0\n"
  "ds_read_b128 v[88:91], v2 offset:8192\n"
  "ds_read_b128 v[92:95], v2 offset:9216\n"
  "v_cvt_pk_bf16_f32 v64, v64, v68\n"
  "v_cvt_pk_bf16_f32 v65, v65, v69\n"
  "v_smfmac_f32_16x16x64_bf16 v[84:87], v[32:35], v[96:103], v0\n"
  "ds_read_b128 v[96:99], v2 offset:10240\n"
  "ds_read_b128 v[100:103], v2 offset:11264\n"
  "ds_write2_b32 v4, v64, v65 offset0:0 offset1:72\n"
  "v_smfmac_f32_16x16x64_bf16 v[72:75], v[36:39], a[48:55], v0\n"
  "v_smfmac_f32_16x16x64_bf16 v[76:79], v[36:39], a[56:63], v0\n"
  "v_smfmac_f32_16x16x64_bf16 v[80:83], v[36:39], v[104:111], v0\n"
  "ds_read_b128 v[104:107], v2 offset:12288\n"
  "ds_read_b128 v[108:111], v2 offset:13312\n"
  "v_smfmac_f32_16x16x64_bf16 v[84:87], v[36:39], v[112:119], v0\n"
  "ds_read_b128 v[112:115], v2 offset:14336\n"
  "ds_read_b128 v[116:119], v2 offset:15360\n"
  "v_smfmac_f32_16x16x64_bf16 v[72:75], v[40:43], a[80:87], v0\n"
  "s_waitcnt lgkmcnt(4)\n"
  "s_barrier\n"
  "ds_read_b128 v[64:67], v5\n"
  "ds_read_b128 v[68:71], v5 offset:64\n"
  "ds_read_b128 v[32:35], v5 offset:128\n"
  "ds_read_b128 v[36:39], v5 offset:192\n"
  "v_smfmac_f32_16x16x64_bf16 v[76:79], v[40:43], a[88:95], v0\n"
  "v_smfmac_f32_16x16x64_bf16 v[80:83], v[40:43], v[88:95], v0\n"
  "ds_read_b128 v[88:91], v3\n"
  "ds_read_b128 v[92:95], v3 offset:1024\n"
  "v_smfmac_f32_16x16x64_bf16 v[84:87], v[40:43], v[96:103], v0\n"
  "ds_read_b128 v[96:99], v3 offset:2048\n"
  "ds_read_b128 v[100:103], v3 offset:3072\n"
  "ds_read_b128 v[40:43], v1 offset:10240\n"
  "v_smfmac_f32_16x16x64_bf16 v[72:75], v[44:47], a[112:119], v0\n"
  "v_smfmac_f32_16x16x64_bf16 v[76:79], v[44:47], a[120:127], v0\n"
  "s_waitcnt lgkmcnt(11)\n"
  "v_smfmac_f32_16x16x64_bf16 v[80:83], v[44:47], v[104:111], v0\n"
  "s_waitcnt lgkmcnt(9)\n"
  "v_smfmac_f32_16x16x64_bf16 v[84:87], v[44:47], v[112:119], v0\n"
  "ds_read_b128 v[44:47], v1 offset:14336\n"
  "ds_read_b128 v[104:107], v1 offset:11264\n"
  "ds_read_b128 v[112:115], v1 offset:15360\n"
  "v_smfmac_f32_16x16x64_bf16 v[72:75], v[48:51], a[144:151], v0\n"
  "v_smfmac_f32_16x16x64_bf16 v[76:79], v[48:51], a[152:159], v0\n"
  "v_smfmac_f32_16x16x64_bf16 v[72:75], v[52:55], a[176:183], v0\n"
  "v_smfmac_f32_16x16x64_bf16 v[76:79], v[52:55], a[184:191], v0\n"
  "s_waitcnt lgkmcnt(6)\n"
  "v_mfma_f32_16x16x32_bf16 v[56:59], v[56:59], v[64:67], 0\n"
  "v_mfma_f32_16x16x32_bf16 v[60:63], v[60:63], v[64:67], 0\n"
  "v_mfma_f32_16x16x32_bf16 v[56:59], v[120:123], v[68:71], v[56:59]\n"
  "v_pk_add_f32 v[72:73], v[72:73], v[74:75]\n"
  "v_pk_add_f32 v[76:77], v[76:77], v[78:79]\n"
  "v_mfma_f32_16x16x32_bf16 v[60:63], v[124:127], v[68:71], v[60:63]\n"
  "v_cvt_pk_bf16_f32 v72, v72, v76\n"
  "v_cvt_pk_bf16_f32 v73, v73, v77\n"
  "s_waitcnt lgkmcnt(3)\n"
  "v_mfma_f32_16x16x32_bf16 v[56:59], v[40:43], v[32:35], v[56:59]\n"
  "ds_write2_b32 v22, v72, v73 offset0:0 offset1:72\n"
  "s_waitcnt lgkmcnt(3)\n"
  "v_mfma_f32_16x16x32_bf16 v[60:63], v[44:47], v[32:35], v[60:63]\n"
  "s_waitcnt lgkmcnt(2)\n"
  "v_mfma_f32_16x16x32_bf16 v[56:59], v[104:107], v[36:39], v[56:59]\n"
  "s_waitcnt lgkmcnt(1)\n"
  "v_mfma_f32_16x16x32_bf16 v[60:63], v[112:115], v[36:39], v[60:63]\n"
  "v_smfmac_f32_16x16x64_bf16 v[80:83], v[48:51], v[88:95], v0\n"
  "ds_read_b128 v[88:91], v3 offset:4096\n"
  "ds_read_b128 v[92:95], v3 offset:5120\n"
  "v_smfmac_f32_16x16x64_bf16 v[84:87], v[48:51], v[96:103], v0\n"
  "ds_read_b128 v[96:99], v3 offset:6144\n"
  "ds_read_b128 v[100:103], v3 offset:7168\n"
  "s_waitcnt lgkmcnt(4)\n"
  "s_barrier\n"
  "ds_read_b128 v[72:75], v5 offset:2304\n"
  "ds_read_b128 v[76:79], v5 offset:2368\n"
  "ds_read_b128 v[120:123], v5 offset:2432\n"
  "ds_read_b128 v[124:127], v5 offset:2496\n"
  "ds_read_b128 v[40:43], v1 offset:22528\n"
  "ds_read_b128 v[44:47], v1 offset:23552\n"
  "s_mov_b32 m0, 145408\n"
  "s_nop 0\n"
  "global_load_lds_dwordx4 v7, s[60:61] sc1\n"
  "global_load_lds_dwordx4 v7, s[60:61] offset:1024 sc1\n"
  "v_max3_f32 v104, v56, v57, v58\n"
  "v_max3_f32 v105, v59, v60, v61\n"
  "v_max3_f32 v104, v104, v62, v63\n"
  "v_max_f32 v104, v104, v105\n"
  "s_waitcnt lgkmcnt(8)\n"
  "v_smfmac_f32_16x16x64_bf16 v[80:83], v[52:55], v[88:95], v0\n"
  "s_waitcnt lgkmcnt(6)\n"
  "v_smfmac_f32_16x16x64_bf16 v[84:87], v[52:55], v[96:103], v0\n"
  "s_waitcnt lgkmcnt(5)\n"
  "v_mfma_f32_16x16x32_bf16 v[112:115], v[72:75], v[64:67], 0\n"
  "s_waitcnt lgkmcnt(4)\n"
  "v_mfma_f32_16x16x32_bf16 v[112:115], v[76:79], v[68:71], v[112:115]\n"
  "s_waitcnt lgkmcnt(3)\n"
  "v_mfma_f32_16x16x32_bf16 v[112:115], v[120:123], v[32:35], v[112:115]\n"
  "s_waitcnt lgkmcnt(2)\n"
  "v_mfma_f32_16x16x32_bf16 v[112:115], v[124:127], v[36:39], v[112:115]\n"
  "s_mov_b64 exec, s[70:71]\n"
  "buffer_store_dwordx4 v[124:127], v10, s[52:55], s64 offen offset:1536 sc1\n"
  "s_mov_b64 exec, -1\n"
  "s_nop 3\n"
  "v_max3_f32 v104, v104, v112, v113\n"
  "v_mov_b32 v105, v104\n"
  "s_nop 1\n"
  "v_permlane32_swap_b32 v104, v105\n"
  "v_max_f32 v104, v104, v105\n"
  "v_mov_b32 v105, v104\n"
  "s_nop 1\n"
  "v_permlane16_swap_b32 v104, v105\n"
  "v_max_f32 v104, v104, v105\n"
  "v_mul_f32 v104, s72, v104\n"
  "v_pk_fma_f32 v[56:57], v[56:57], s[72:73], v[104:105] op_sel_hi:[1,0,0] neg_lo:[0,0,1] neg_hi:[0,0,1]\n"
  "v_pk_fma_f32 v[58:59], v[58:59], s[72:73], v[104:105] op_sel_hi:[1,0,0] neg_lo:[0,0,1] neg_hi:[0,0,1]\n"
  "v_pk_fma_f32 v[60:61], v[60:61], s[72:73], v[104:105] op_sel_hi:[1,0,0] neg_lo:[0,0,1] neg_hi:[0,0,1]\n"
  "v_pk_fma_f32 v[62:63], v[62:63], s[72:73], v[104:105] op_sel_hi:[1,0,0] neg_lo:[0,0,1] neg_hi:[0,0,1]\n"
  "v_exp_f32 v56, v56\n"
  "v_exp_f32 v57, v57\n"
  "v_exp_f32 v58, v58\n"
  "v_exp_f32 v59, v59\n"
  "v_exp_f32 v60, v60\n"
  "v_exp_f32 v61, v61\n"
  "v_exp_f32 v62, v62\n"
  "v_exp_f32 v63, v63\n"
  "v_cvt_pk_bf16_f32 v32, v56, v57\n"
  "v_cvt_pk_bf16_f32 v33, v58, v59\n"
  "v_cvt_pk_bf16_f32 v34, v60, v61\n"
  "v_cvt_pk_bf16_f32 v35, v62, v63\n"
  "v_pk_fma_f32 v[112:113], v[112:113], s[72:73], v[104:105] op_sel_hi:[1,0,0] neg_lo:[0,0,1] neg_hi:[0,0,1]\n"
  "s_nop 0\n"
  "v_mfma_f32_16x16x32_bf16 v[36:39], v[18:21], v[32:35], 0\n"
  "v_exp_f32 v112, v112\n"
  "v_exp_f32 v113, v113\n"
  "s_waitcnt lgkmcnt(0)\n"
  "v_mfma_f32_16x16x32_bf16 v[64:67], v[40:43], v[32:35], 0\n"
  "v_cvt_pk_bf16_f32 v24, v80, v81\n"
  "v_cvt_pk_bf16_f32 v25, v82, v83\n"
  "v_mfma_f32_16x16x32_bf16 v[68:71], v[44:47], v[32:35], 0\n"
  "v_cvt_pk_bf16_f32 v28, v112, v113\n"
  "v_cvt_pk_bf16_f32 v29, v112, v113\n"
  "v_cvt_pk_bf16_f32 v26, v84, v85\n"
  "v_cvt_pk_bf16_f32 v27, v86, v87\n"
  "v_mfma_f32_16x16x32_bf16 v[36:39], v[16:19], v[28:31], v[36:39]\n"
  "v_mfma_f32_16x16x32_bf16 v[64:67], v[24:27], v[28:31], v[64:67]\n"
  "v_mfma_f32_16x16x32_bf16 v[68:71], v[26:29], v[28:31], v[68:71]\n"
  "v_mov_b64 v[48:49], 0\n"
  "v_mov_b64 v[50:51], 0\n"
  "v_mov_b64 v[52:53], 0\n"
  "v_mov_b64 v[54:55], 0\n"
  "v_rcp_f32 v106, v36\n"
  "v_mov_b64 v[56:57], 0\n"
  "v_pk_mul_f32 v[64:65], v[64:65], v[106:107] op_sel_hi:[1,0]\n"
  "v_pk_mul_f32 v[66:67], v[66:67], v[106:107] op_sel_hi:[1,0]\n"
  "v_pk_mul_f32 v[68:69], v[68:69], v[106:107] op_sel_hi:[1,0]\n"
  "v_pk_mul_f32 v[70:71], v[70:71], v[106:107] op_sel_hi:[1,0]\n"
  "v_cvt_pk_bf16_f32 v84, v64, v65\n"
  "v_cvt_pk_bf16_f32 v85, v66, v67\n"
  "v_cvt_pk_bf16_f32 v86, v68, v69\n"
  "v_cvt_pk_bf16_f32 v87, v70, v71\n"
  "ds_write_b128 v6, v[84:87]\n"
  "buffer_store_dwordx2 v[24:25], v11, s[52:55], s65 offen sc1\n"
  "buffer_store_dwordx2 v[26:27], v11, s[52:55], s65 offen offset:512 sc1\n"
  "s_cmp_eq_u32 s62, 63\n"
  "s_cbranch_scc1 CH_exit_3_%=\n"
  "v_mov_b64 v[58:59], 0\n"
  "v_mov_b64 v[60:61], 0\n"
  "v_mov_b64 v[62:63], 0\n"
  "s_waitcnt lgkmcnt(0)\n"
  "s_barrier\n"
  "ds_read_b128 v[88:91], v1 offset:29184\n"
  "ds_read_b128 v[92:95], v1 offset:30208\n"
  "v_mov_b64 v[32:33], 0\n"
  "v_mov_b64 v[34:35], 0\n"
  "v_mov_b64 v[36:37], 0\n"
  "v_mov_b64 v[38:39], 0\n"
  "v_mov_b64 v[40:41], 0\n"
  "v_mov_b64 v[42:43], 0\n"
  "v_mov_b64 v[44:45], 0\n"
  "v_mov_b64 v[46:47], 0\n"
  "s_add_u32 s84, s66, 0\n"
  "s_add_u32 s85, s66, 32768\n"
  "s_add_u32 s86, s66, 65536\n"
  "s_add_u32 s87, s66, 98304\n"
  "s_mov_b32 m0, 153600\n"
  "s_nop 0\n"
  "global_load_lds_dwordx4 v7, s[50:51] sc1\n"
  "global_load_lds_dwordx4 v7, s[50:51] offset:1024 sc1\n"
  "s_waitcnt lgkmcnt(1)\n"
  "v_smfmac_f32_16x16x64_bf16 v[32:35], v[88:91], v[128:135], v0\n"
  "v_smfmac_f32_16x16x64_bf16 v[36:39], v[88:91], v[144:151], v0\n"
  "s_waitcnt lgkmcnt(0)\n"
  "v_smfmac_f32_16x16x64_bf16 v[32:35], v[92:95], v[136:143], v0\n"
  "v_smfmac_f32_16x16x64_bf16 v[36:39], v[92:95], v[152:159], v0\n"
  "v_smfmac_f32_16x16x64_bf16 v[40:43], v[88:91], v[160:167], v0\n"
  "v_smfmac_f32_16x16x64_bf16 v[44:47], v[88:91], v[176:183], v0\n"
  "v_pk_add_f32 v[32:33], v[32:33], v[34:35]\n"
  "v_pk_add_f32 v[36:37], v[36:37], v[38:39]\n"
  "v_smfmac_f32_16x16x64_bf16 v[40:43], v[92:95], v[168:175], v0\n"
  "v_cvt_pk_bf16_f32 v32, v32, v33\n"
  "v_cvt_pk_bf16_f32 v33, v36, v37\n"
  "v_smfmac_f32_16x16x64_bf16 v[44:47], v[92:95], v[184:191], v0\n"
  "v_permlane16_swap_b32 v32, v33\n"
  "buffer_store_dwordx2 v[32:33], v8, s[56:59], s84 offen sc1\n"
  "v_smfmac_f32_16x16x64_bf16 v[48:51], v[88:91], v[192:199], v0\n"
  "v_smfmac_f32_16x16x64_bf16 v[52:55], v[88:91], v[208:215], v0\n"
  "v_pk_add_f32 v[40:41], v[40:41], v[42:43]\n"
  "v_pk_add_f32 v[44:45], v[44:45], v[46:47]\n"
  "v_smfmac_f32_16x16x64_bf16 v[48:51], v[92:95], v[200:207], v0\n"
  "v_cvt_pk_bf16_f32 v40, v40, v41\n"
  "v_cvt_pk_bf16_f32 v41, v44, v45\n"
  "v_smfmac_f32_16x16x64_bf16 v[52:55], v[92:95], v[216:223], v0\n"
  "v_permlane16_swap_b32 v40, v41\n"
  "buffer_store_dwordx2 v[40:41], v8, s[56:59], s85 offen sc1\n"
  "v_smfmac_f32_16x16x64_bf16 v[56:59], v[88:91], v[224:231], v0\n"
  "v_smfmac_f32_16x16x64_bf16 v[60:63], v[88:91], v[240:247], v0\n"
  "v_pk_add_f32 v[48:49], v[48:49], v[50:51]\n"
  "v_pk_add_f32 v[52:53], v[52:53], v[54:55]\n"
  "v_smfmac_f32_16x16x64_bf16 v[56:59], v[92:95], v[232:239], v0\n"
  "v_cvt_pk_bf16_f32 v48, v48, v49\n"
  "v_cvt_pk_bf16_f32 v49, v52, v53\n"
  "v_smfmac_f32_16x16x64_bf16 v[60:63], v[92:95], v[248:255], v0\n"
  "v_permlane16_swap_b32 v48, v49\n"
  "buffer_store_dwordx2 v[48:49], v8, s[56:59], s86 offen sc1\n"
  "v_pk_add_f32 v[56:57], v[56:57], v[58:59]\n"
  "s_nop 2\n"
  "v_pk_add_f32 v[60:61], v[60:61], v[62:63]\n"
  "v_cvt_pk_bf16_f32 v56, v56, v57\n"
  "v_cvt_pk_bf16_f32 v57, v60, v61\n"
  "s_nop 1\n"
  "v_permlane16_swap_b32 v56, v57\n"
  "buffer_store_dwordx2 v[56:57], v8, s[56:59], s87 offen sc1\n"
  "v_mov_b32 v36, -1\n"
  "v_mov_b32 v37, -1\n"
  "v_mov_b32 v38, -1\n"
  "v_mov_b32 v39, -1\n"
  "buffer_store_dwordx4 v[36:39], v7, s[56:59], s90 offen sc1\n"
  "buffer_store_dwordx4 v[36:39], v7, s[56:59], s90 offen offset:1024 sc1\n"
  "ds_read_b128 v[88:91], v3 offset:8192\n"
  "ds_read_b128 v[92:95], v3 offset:9216\n"
  "ds_read_b128 v[96:99], v3 offset:10240\n"
  "ds_read_b128 v[100:103], v3 offset:11264\n"
  "ds_read_b128 v[104:107], v3 offset:12288\n"
  "ds_read_b128 v[108:111], v3 offset:13312\n"
  "ds_read_b128 v[112:115], v3 offset:14336\n"
  "ds_read_b128 v[116:119], v3 offset:15360\n"
  "v_mov_b64 v[64:65], 0\n"
  "v_mov_b64 v[66:67], 0\n"
  "v_mov_b64 v[68:69], 0\n"
  "v_mov_b64 v[70:71], 0\n"
  "v_mov_b64 v[72:73], 0\n"
  "v_mov_b64 v[74:75], 0\n"
  "v_mov_b64 v[76:77], 0\n"
  "v_mov_b64 v[78:79], 0\n"
  "v_mov_b64 v[80:81], 0\n"
  "v_mov_b64 v[82:83], 0\n"
  "v_mov_b64 v[84:85], 0\n"
  "v_mov_b64 v[86:87], 0\n"
  "s_add_u32 s62, s62, 1\n"
  "s_add_u32 s63, s63, 4\n"
  "s_add_u32 s64, s64, 262144\n"
  "s_add_u32 s65, s65, 262144\n"
  "s_add_u32 s66, s66, 0x80000\n"
  "s_and_b32 s66, s66, 0x1fffff\n"
  "s_add_u32 s90, s90, 0x80000\n"
  "s_and_b32 s90, s90, 0x1fffff\n"
  "s_cmp_lt_u32 s62, 63\n"
  "s_cselect_b32 s74, 524288, 0\n"
  "s_add_u32 s50, s50, s74\n"
  "s_addc_u32 s51, s51, 0\n"
  "s_add_u32 s60, s60, s74\n"
  "s_addc_u32 s61, s61, 0\n"
  "s_waitcnt lgkmcnt(0)\n"
  "s_barrier\n"
  "s_sleep 4\n"
  "s_nop 3\n"
  "s_mov_b32 s73, 200000\n"
  "s_add_u32 s91, s91, 16384\n"
  "s_branch CH_p2_3_%=\n"
  "CH_p2retry_3_%=:\n"
  "s_waitcnt vmcnt(0)\n"
  "v_max3_u32 v40, v120, v122, v124\n"
  "v_max_u32 v40, v40, v126\n"
  "v_cmp_eq_u32 vcc, -1, v40\n"
  "s_cbranch_vccnz CH_p2again_3_%=\n"
  "v_mov_b64 v[56:57], v[120:121]\n"
  "v_mov_b64 v[58:59], v[122:123]\n"
  "v_mov_b64 v[60:61], v[124:125]\n"
  "v_mov_b64 v[62:63], v[126:127]\n"
  "s_branch CH_p2ok_3_%=\n"
  "CH_p2again_3_%=:\n"
  "s_sub_u32 s73, s73, 1\n"
  "s_cbranch_scc0 CH_p2_3_%=\n"
  "s_branch CH_END_%=\n"
  "CH_exit_3_%=:\n"
  "s_waitcnt vmcnt(0)\n"
  "s_barrier\n"
  "s_add_u32 s63, s63, 4\n"
  "s_mov_b64 exec, 1\n"
  "buffer_store_dword v0, off, s[56:59], s63 sc1\n"
  "s_mov_b64 exec, -1\n"
  "s_waitcnt vmcnt(0)\n"
  "s_branch CH_END_%=\n"
  "CH_END_%=:\n"
  "s_waitcnt vmcnt(0) lgkmcnt(0)\n"
      :
      : "{s[40:41]}"(wq), "{s[42:43]}"(wk), "{s[44:45]}"(wv), "{s[46:47]}"(wo), "{s[48:49]}"(kv), "{s[52:53]}"(kvm), "{s[56:57]}"(ch),
        "{s[80:81]}"(P.mem0), "{s[82:83]}"(stp), "{s68}"(w), "{s69}"(h),
        "{v[0:15]}"(c0), "{v[16:19]}"(c1), "{v[20:21]}"(c1b), "{v[96:111]}"(c2), "{v112}"(m0off)
      :
        "memory", "vcc", "v22", "v24", "v25", "v26", "v27", "v28", "v29", "v30", "v31", "v32", "v33", "v34", "v35", "v36", "v37", "v38", "v39", "v40", "v41", "v42", "v43", "v44",
        "v45", "v46", "v47", "v48", "v49", "v50", "v51", "v52", "v53", "v54", "v55", "v56", "v57", "v58", "v59", "v60", "v61", "v62", "v63", "v64", "v65", "v66", "v67", "v68",
        "v69", "v70", "v71", "v72", "v73", "v74", "v75", "v76", "v77", "v78", "v79", "v80", "v81", "v82", "v83", "v84", "v85", "v86", "v87", "v88", "v89", "v90", "v91", "v92",
        "v93", "v94", "v95", "v113", "v114", "v115", "v116", "v117", "v118", "v119", "v120", "v121", "v122", "v123", "v124", "v125", "v126", "v127", "v128", "v129", "v130", "v131", "v132", "v133",
        "v134", "v135", "v136", "v137", "v138", "v139", "v140", "v141", "v142", "v143", "v144", "v145", "v146", "v147", "v148", "v149", "v150", "v151", "v152", "v153", "v154", "v155", "v156", "v157",
        "v158", "v159", "v160", "v161", "v162", "v163", "v164", "v165", "v166", "v167", "v168", "v169", "v170", "v171", "v172", "v173", "v174", "v175", "v176", "v177", "v178", "v179", "v180", "v181",
        "v182", "v183", "v184", "v185", "v186", "v187", "v188", "v189", "v190", "v191", "v192", "v193", "v194", "v195", "v196", "v197", "v198", "v199", "v200", "v201", "v202", "v203", "v204", "v205",
        "v206", "v207", "v208", "v209", "v210", "v211", "v212", "v213", "v214", "v215", "v216", "v217", "v218", "v219", "v220", "v221", "v222", "v223", "v224", "v225", "v226", "v227", "v228", "v229",
        "v230", "v231", "v232", "v233", "v234", "v235", "v236", "v237", "v238", "v239", "v240", "v241", "v242", "v243", "v244", "v245", "v246", "v247", "v248", "v249", "v250", "v251", "v252", "v253",
        "v254", "v255", "a0", "a1", "a2", "a3", "a4", "a5", "a6", "a7", "a8", "a9", "a10", "a11", "a12", "a13", "a14", "a15", "a16", "a17", "a18", "a19", "a20", "a21",
        "a22", "a23", "a24", "a25", "a26", "a27", "a28", "a29", "a30", "a31", "a32", "a33", "a34", "a35", "a36", "a37", "a38", "a39", "a40", "a41", "a42", "a43", "a44", "a45",
        "a46", "a47", "a48", "a49", "a50", "a51", "a52", "a53", "a54", "a55", "a56", "a57", "a58", "a59", "a60", "a61", "a62", "a63", "a64", "a65", "a66", "a67", "a68", "a69",
        "a70", "a71", "a72", "a73", "a74", "a75", "a76", "a77", "a78", "a79", "a80", "a81", "a82", "a83", "a84", "a85", "a86", "a87", "a88", "a89", "a90", "a91", "a92", "a93",
        "a94", "a95", "a96", "a97", "a98", "a99", "a100", "a101", "a102", "a103", "a104", "a105", "a106", "a107", "a108", "a109", "a110", "a111", "a112", "a113", "a114", "a115", "a116", "a117",
        "a118", "a119", "a120", "a121", "a122", "a123", "a124", "a125", "a126", "a127", "a128", "a129", "a130", "a131", "a132", "a133", "a134", "a135", "a136", "a137", "a138", "a139", "a140", "a141",
        "a142", "a143", "a144", "a145", "a146", "a147", "a148", "a149", "a150", "a151", "a152", "a153", "a154", "a155", "a156", "a157", "a158", "a159", "a160", "a161", "a162", "a163", "a164", "a165",
        "a166", "a167", "a168", "a169", "a170", "a171", "a172", "a173", "a174", "a175", "a176", "a177", "a178", "a179", "a180", "a181", "a182", "a183", "a184", "a185", "a186", "a187", "a188", "a189",
        "a190", "a191", "a192", "a193", "a194", "a195", "a196", "a197", "a198", "a199", "a200", "a201", "a202", "a203", "a204", "a205", "a206", "a207", "a208", "a209", "a210", "a211", "a212", "a213",
        "a214", "a215", "a216", "a217", "a218", "a219", "a220", "a221", "a222", "a223", "a224", "a225", "a226", "a227", "a228", "a229", "a230", "a231", "a232", "a233", "a234", "a235", "a236", "a237",
        "a238", "a239", "a240", "a241", "a242", "a243", "a244", "a245", "a246", "a247", "a248", "a249", "a250", "a251", "a252", "a253", "a254", "a255", "s50", "s51", "s54", "s55", "s58", "s59",
        "s60", "s61", "s62", "s63", "s64", "s65", "s66", "s67", "s70", "s71", "s72", "s73", "s74", "s75", "s76", "s77", "s78", "s79", "s84", "s85", "s86", "s87", "s88", "s89",
        "s90", "s91", "s92", "s93", "s94", "s95", "s96", "s97", "s98", "s99"
  );
}

constexpr u32 AL_H = 0  , AL_KV = 66560  , AL_KVM = 82944  , AL_EF = 89088  , AL_Q = 90112  , AL_O = 98304  ;
DEV void role_att(const Params& P, int h, int s0, int ds, ldsp lds) {
  const int t = threadIdx.x, l = t & 63, w = rfl(t >> 6);
  const int rho = l & 15, g = l >> 4;
  u4 Wq[2][16], Wo[8][4];
  const int LD = NH * DH;
  {
    const __hip_bfloat16* q = P.Wq + h * DH;
    const __hip_bfloat16* o = P.Wo + (size_t)h * DH * DM;
    auto issue = [&](int u) {
      u32 off = (u & 1) * 65536;
      if (u < 2) stage_block<256>(q + (size_t)u * 256 * LD, LD, off, w, l);
      else {
#pragma unroll
        for (int b = 0; b < 4; ++b) stage_block<64>(o + (size_t)(u - 2) * 64 * DM + b * 128, DM, off + b * 16384, w, l);
      }
    };
    issue(0);
#pragma unroll
    for (int u = 0; u < 4; ++u) {
      if (u < 3) { issue(u + 1); asm volatile("s_waitcnt vmcnt(16)" ::: "memory"); } else wait_vm0();
      bar();
      ldsp blk = lds + (u & 1) * 65536;
      if (u < 2) {
#pragma unroll
        for (int tt = 0; tt < 2; ++tt)
#pragma unroll
          for (int k8 = 0; k8 < 8; ++k8) Wq[tt][u * 8 + k8] = dense_frag(blk, k8 * 32, 32 * w + 16 * tt + 4 * (l & 3), l);
      } else {
#pragma unroll
        for (int T = 0; T < 8; ++T)
#pragma unroll
          for (int k2 = 0; k2 < 2; ++k2) Wo[T][(u - 2) * 2 + k2] = dense_frag(blk + w * 16384, k2 * 32, 32 * (T >> 1) + 8 * (l & 3) + 4 * (T & 1), l);
      }
      bar();
    }
  }
  const char* H = P.ws + OFF_H;
  const char* KV = P.ws + OFF_KV;
  const char* KVM = P.ws + OFF_KVM;
  rsrc_t rp2 = mkrsrc(P.ws + OFF_P2, 0x4000000);
  const float cexp = 0.08838834764831845f * 1.4426950408889634f;
  const int qt = w & 1, eh = w >> 1;
  const char* fl = flag_base(P);
  int buf = 0, kvok = 0;
  Pred pq;
  pred_init(pq);
  if (w == 0) {
#pragma unroll 1
    for (int p = 0; p < 8; ++p) wait_words(fl + F_H + p * 256, 64, l);
  }
  bar();
  stage_rows<true>(H + (size_t)s0 * 32768, AL_H, w, l);
  for (int s = s0; s < NSEG; s += ds) {
    if (s >= kvok) {
      wait_words(fl + F_KV + h * 512 + s * 4, 1, l);
      u64 nr = __builtin_amdgcn_ballot_w64(ld_flag(fl + F_KV + h * 512 + l * 4) == 0xffffffffu);
      kvok = nr ? __builtin_ctzll(nr) : 64;
    }
    if (s == s0) { wait_vm0(); bar(); tl(P, 2); }
#pragma unroll
    for (int i = 0; i < 4; ++i) glds16_sc1(KV + (size_t)(s * NH + h) * 16384 + (w * 4 + i) * 1024 + l * 16, AL_KV + (u32)(w * 4 + i) * 1024u);
    if (t == 0) glds16_sc1(fl + F_KV + h * 512 + 256 + (s & ~3) * 4, AL_EF);
    if (s + ds < NSEG) stage_rows<true, PACEA>(H + (size_t)(s + ds) * 32768, AL_H + (buf ^ 1) * HBUF, w, l);
    asm volatile("" ::: "memory");
    ldsp hb = lds + AL_H + buf * HBUF + rho * HROW + g * 16;
    f4 qa[2][2] = {{{0, 0, 0, 0}, {0, 0, 0, 0}}, {{0, 0, 0, 0}, {0, 0, 0, 0}}};
#pragma unroll
    for (int kk = 0; kk < 16; ++kk) {
      u4 h0 = lds_r128(hb + kk * 64), h1 = lds_r128(hb + 16 * HROW + kk * 64);
#pragma unroll
      for (int tt = 0; tt < 2; ++tt) { qa[tt][0] = mfma32(Wq[tt][kk], h0, qa[tt][0]); qa[tt][1] = mfma32(Wq[tt][kk], h1, qa[tt][1]); }
    }
#pragma unroll
    for (int q2 = 0; q2 < 2; ++q2) {
      f4 a = qa[0][q2], b = qa[1][q2];
      lds_w128(lds + AL_Q + (q2 * 4 + w) * 1024 + l * 16, (u4){cvtpk(a[0], b[0]), cvtpk(a[1], b[1]), cvtpk(a[2], b[2]), cvtpk(a[3], b[3])});
    }
    wait_vm0();
    bar();
    if (s > s0 && t < nred(s - ds)) st_flag(fl + F_P2 + (s - ds) * 4096 + t * 128 + h * 4, 1u);
    if (s > s0) tl(P, 8 + s - ds);
    bool early = false;
    if (w == 0) {
      early = rfl(*(__attribute__((address_space(3))) u32*)(lds + AL_EF + (s & 3) * 4)) != 0xffffffffu;
      if (early) {
#pragma unroll
        for (int f = 0; f < 6; ++f) glds16_sc1(KVM + (size_t)(s * NH + h) * 8192 + f * 1024 + l * 16, AL_KVM + (u32)f * 1024u);
      }
    }
    if ((s >> 1) == SEGT) tl(P, 73);
    u4 qf[4];
    f4 st[2] = {{0, 0, 0, 0}, {0, 0, 0, 0}};
#pragma unroll
    for (int kk = 0; kk < 4; ++kk) {
      qf[kk] = lds_r128(lds + AL_Q + (qt * 4 + kk) * 1024 + l * 16);
      u4 k0 = lds_r128(lds + AL_KV + kk * 1024 + l * 16), k1 = lds_r128(lds + AL_KV + (4 + kk) * 1024 + l * 16);
      st[0] = mfma32(k0, qf[kk], st[0]);
      st[1] = mfma32(k1, qf[kk], st[1]);
    }
    const int qtok = 8 * (rho >> 2) + 4 * qt + (rho & 3);
    float m1 = -3e38f;
#pragma unroll
    for (int a = 0; a < 2; ++a)
#pragma unroll
      for (int c = 0; c < 4; ++c) {
        float v = st[a][c] * cexp;
        if ((8 * g + 4 * a + c) > qtok) v = -1e9f * 1.4426950408889634f;
        st[a][c] = v;
        m1 = fmaxf(m1, v);
      }
    m1 = red4_max(m1);
    float sum = 0;
#pragma unroll
    for (int a = 0; a < 2; ++a)
#pragma unroll
      for (int c = 0; c < 4; ++c) { st[a][c] = __builtin_amdgcn_exp2f(st[a][c] - m1); sum += st[a][c]; }
    const u4 pt = {cvtpk(st[0][0], st[0][1]), cvtpk(st[0][2], st[0][3]), cvtpk(st[1][0], st[1][1]), cvtpk(st[1][2], st[1][3])};
    f4 oa[4];
#pragma unroll
    for (int e4 = 0; e4 < 4; ++e4) oa[e4] = mfma32(lds_r128(lds + AL_KV + 8192 + (eh * 4 + e4) * 1024 + l * 16), pt, (f4){0, 0, 0, 0});
    if (w == 0) {
      if (!early) {
        wait_words(fl + F_KV + h * 512 + 256 + s * 4, 1, l);
        if ((s >> 1) == SEGT) tl(P, 74);
#pragma unroll
        for (int f = 0; f < 6; ++f) glds16_sc1(KVM + (size_t)(s * NH + h) * 8192 + f * 1024 + l * 16, AL_KVM + (u32)f * 1024u);
      }
      wait_vm0();
    }
    bar();
    if ((s >> 1) == SEGT) tl(P, 75);
    f4 sm = {0, 0, 0, 0}, sm2 = {0, 0, 0, 0};
#pragma unroll
    for (int kk = 0; kk < 4; kk += 2) {
      sm = mfma32(lds_r128(lds + AL_KVM + kk * 512 + (g * 8 + mrow(rho)) * 16), qf[kk], sm);
      sm2 = mfma32(lds_r128(lds + AL_KVM + (kk + 1) * 512 + (g * 8 + mrow(rho)) * 16), qf[kk + 1], sm2);
    }
    u2 vm[4];
#pragma unroll
    for (int e4 = 0; e4 < 4; ++e4) vm[e4] = *(__attribute__((address_space(3))) u2*)(lds + AL_KVM + 2048 + (eh * 4 + e4) * 512 + l * 8);
    const float s0 = (sm[0] + sm2[0]) * cexp, s1 = (sm[1] + sm2[1]) * cexp;
    const float mx = fmaxf(m1, red4_max(fmaxf(s0, s1)));
    const float f1 = __builtin_amdgcn_exp2f(m1 - mx), p0 = __builtin_amdgcn_exp2f(s0 - mx), p1 = __builtin_amdgcn_exp2f(s1 - mx);
    const u32 pmw = cvtpk(p0, p1);
    const float inv = __builtin_amdgcn_rcpf(red4_sum(sum * f1 + (p0 + p1)));
#pragma unroll
    for (int e4 = 0; e4 < 4; ++e4) oa[e4] = mfma32((u4){vm[e4].x, vm[e4].y, 0u, 0u}, (u4){pmw, pmw, 0u, 0u}, oa[e4] * f1);
#pragma unroll
    for (int pp = 0; pp < 2; ++pp) {
      f4 a = oa[2 * pp] * inv, b = oa[2 * pp + 1] * inv;
      lds_w128(lds + AL_O + (qt * 4 + eh * 2 + pp) * 1024 + l * 16, (u4){cvtpk(a[0], a[1]), cvtpk(a[2], a[3]), cvtpk(b[0], b[1]), cvtpk(b[2], b[3])});
    }
    bar();
    if ((s >> 1) == SEGT) tl(P, 76);
    u4 of[2][4];
#pragma unroll
    for (int q2 = 0; q2 < 2; ++q2)
#pragma unroll
      for (int kk = 0; kk < 4; ++kk) of[q2][kk] = lds_r128(lds + AL_O + (q2 * 4 + kk) * 1024 + l * 16);
#pragma unroll
    for (int p = 0; p < 4; ++p) {
      f4 a[2] = {{0, 0, 0, 0}, {0, 0, 0, 0}}, b[2] = {{0, 0, 0, 0}, {0, 0, 0, 0}};
#pragma unroll
      for (int kk = 0; kk < 4; ++kk)
#pragma unroll
        for (int q2 = 0; q2 < 2; ++q2) { a[q2] = mfma32(Wo[2 * p][kk], of[q2][kk], a[q2]); b[q2] = mfma32(Wo[2 * p + 1][kk], of[q2][kk], b[q2]); }
#pragma unroll
      for (int q2 = 0; q2 < 2; ++q2) {
        int tok = 8 * (rho >> 2) + 4 * q2 + (rho & 3);
        bstore128(rp2, (u32)(s * NH + h) * 32768u + tok * 1024 + (128 * w + 32 * p + 8 * g) * 2,
                  (u4){cvtpk(a[q2][0], a[q2][1]), cvtpk(a[q2][2], a[q2][3]), cvtpk(b[q2][0], b[q2][1]), cvtpk(b[q2][2], b[q2][3])});
      }
    }
    if ((s >> 1) == SEGT) tl(P, 77);
    buf ^= 1;
  }
  wait_vm0();
  bar();
  if (t < nred(NSEG - ds + s0)) st_flag(fl + F_P2 + (NSEG - ds + s0) * 4096 + t * 128 + h * 4, 1u);
  tl(P, 8 + NSEG - ds + s0);
  (void)cexp;
}

struct RedC { float ga[8], sb[8], cb[8]; };
DEV void red_consts(const Params& P, int l, RedC& c) {
  rsrc_t rm = mkrsrc(P.ws + OFF_MOD, 3072 * 4);
  u4 ga0 = bload128(rm, 4096 + l * 32), ga1 = bload128(rm, 4096 + l * 32 + 16);
  u4 sb0 = bload128(rm, 6144 + l * 32), sb1 = bload128(rm, 6144 + l * 32 + 16);
  u4 cb0 = bload128(rm, 8192 + l * 32), cb1 = bload128(rm, 8192 + l * 32 + 16);
#pragma unroll
  for (int j = 0; j < 8; ++j) {
    c.ga[j] = asf(j < 4 ? ga0[j & 3] : ga1[j & 3]);
    c.sb[j] = asf(j < 4 ? sb0[j & 3] : sb1[j & 3]);
    c.cb[j] = bfr(1.f + asf(j < 4 ? cb0[j & 3] : cb1[j & 3]));
  }
}
DEV void red_finish(const Params& P, int row, const float (&a)[8], u4 xv, const RedC& c, int l) {
  float v[8];
#pragma unroll
  for (int j = 0; j < 8; ++j) {
    float xx = (j & 1) ? bfhi(xv[j >> 1]) : bflo(xv[j >> 1]);
    v[j] = bfr(xx + bfr(c.ga[j] * bfr(a[j])));
  }
  u4 x1 = {cvtpk(v[0], v[1]), cvtpk(v[2], v[3]), cvtpk(v[4], v[5]), cvtpk(v[6], v[7])};
  bstore128(mkrsrc(P.ws + OFF_X1, S_ * DM * 2), (row * DM + l * 8) * 2, x1);
  float sm = 0;
#pragma unroll
  for (int j = 0; j < 8; ++j) sm += v[j];
  float mean = wave_sum(sm) * (1.f / DM), q = 0;
#pragma unroll
  for (int j = 0; j < 8; ++j) { v[j] -= mean; q += v[j] * v[j]; }
  float rstd = rsqrtf(wave_sum(q) * (1.f / DM) + 1e-5f);
  float hh[8];
#pragma unroll
  for (int j = 0; j < 8; ++j) hh[j] = v[j] * rstd * c.cb[j] + c.sb[j];
  bstore128(mkrsrc(P.ws + OFF_H2, S_ * DM * 2), (row * DM + l * 8) * 2,
            (u4){cvtpk(hh[0], hh[1]), cvtpk(hh[2], hh[3]), cvtpk(hh[4], hh[5]), cvtpk(hh[6], hh[7])});
  wait_vm0();
  if (l < 22) st_flag(flag_base(P) + F_H2 + (row >> 5) * (22 * 128) + l * 128 + (row & 31) * 4, 1u);
}
DEV void role_red_early(const Params& P, int e) {
  const int t = threadIdx.x, l = t & 63, w = rfl(t >> 6);
  const char* fl = flag_base(P);
  wait_words(fl + F_MOD + 32 * 4, 48, l);
  RedC c;
  red_consts(P, l, c);
  rsrc_t rp2 = mkrsrc(P.ws + OFF_P2, 0x4000000);
  const int tok = 4 * (e & 7) + w;
  Pred pq;
  pred_init(pq);
  for (int s = e >> 3; s < NSEG;) {
    const int row = s * 32 + tok;
    u4 xv = *(const u4*)(P.x + (size_t)row * DM + l * 8);
    if (w == 0) {
      wait_pred(fl + F_P2 + s * 4096 + (e & 7) * 128, 32, l, pq);
    }
    bar();
    if ((s >> 1) == SEGT) tl(P, 72);
    u4 d[32];
#pragma unroll
    for (int i = 0; i < 32; ++i) d[i] = bload128(rp2, (u32)(s * NH + i) * 32768u + tok * 1024 + l * 16);
    float a[8] = {0, 0, 0, 0, 0, 0, 0, 0};
#pragma unroll
    for (int i = 0; i < 32; ++i)
#pragma unroll
      for (int j = 0; j < 4; ++j) { a[2 * j] += bflo(d[i][j]); a[2 * j + 1] += bfhi(d[i][j]); }
    if ((s >> 1) == SEGT) tl(P, 73);
    red_finish(P, row, a, xv, c, l);
    tl(P, 8 + s);
    s += 2;
    if (s >= S_SW) { s = (s + 1) & ~1; if (((s >> 1) & 1) != (e >> 3)) s += 2; }
  }
}
DEV void role_red_late(const Params& P, int tok, int set, ldsp lds) {
  const int t = threadIdx.x, l = t & 63, w = rfl(t >> 6);
  const char* fl = flag_base(P);
  RedC c;
  if (w == 0) { wait_words(fl + F_MOD + 32 * 4, 48, l); }
  bar();
  red_consts(P, l, c);
  rsrc_t rp2 = mkrsrc(P.ws + OFF_P2, 0x4000000);
  Pred pq;
  pred_init(pq);
  for (int s = S_SW + 1; s < NSEG; s += 2) {
    const int row = s * 32 + tok;
    u4 xv = *(const u4*)(P.x + (size_t)row * DM + l * 8);
    if (w == 0) {
      wait_pred(fl + F_P2 + s * 4096 + tok * 128, 32, l, pq);
    }
    bar();
    if ((s >> 1) == SEGT) tl(P, 72);
    u4 d[8];
#pragma unroll
    for (int i = 0; i < 8; ++i) d[i] = bload128(rp2, (u32)(s * NH + w * 8 + i) * 32768u + tok * 1024 + l * 16);
    float a[8] = {0, 0, 0, 0, 0, 0, 0, 0};
#pragma unroll
    for (int i = 0; i < 8; ++i)
#pragma unroll
      for (int j = 0; j < 4; ++j) { a[2 * j] += bflo(d[i][j]); a[2 * j + 1] += bfhi(d[i][j]); }
    if (w > 0) {
      lds_wf4(lds + w * 2048 + l * 32, (f4){a[0], a[1], a[2], a[3]});
      lds_wf4(lds + w * 2048 + l * 32 + 16, (f4){a[4], a[5], a[6], a[7]});
    }
    bar();
    if (w == 0) {
#pragma unroll
      for (int ww = 1; ww < 4; ++ww) {
        f4 x = lds_rf4(lds + ww * 2048 + l * 32), y = lds_rf4(lds + ww * 2048 + l * 32 + 16);
#pragma unroll
        for (int j = 0; j < 4; ++j) { a[j] += x[j]; a[4 + j] += y[j]; }
      }
      if ((s >> 1) == SEGT) tl(P, 73);
      red_finish(P, row, a, xv, c, l);
      tl(P, 64 + s);
    }
  }
}

DEV float gelu_tanh(float x) {
  const float k1 = 0.7978845608028654f * 2.8853900817779268f, k3 = k1 * 0.044715f;
  float e = __builtin_amdgcn_exp2f(x * __builtin_fmaf(k3, x * x, k1));
  return __builtin_fmaf(-x, __builtin_amdgcn_rcpf(e + 1.f), x);
}
DEV void role_gu(const Params& P, int j, int set, ldsp lds) {
  const int t = threadIdx.x, l = t & 63, w = rfl(t >> 6);
  const char* fl = flag_base(P);
  u4 Wg[2][16], Wu[2][16];
  {
#pragma unroll 8
    for (int i = 0; i < 32; ++i) {
      int row = (w * 32 + i) * 4 + (l >> 4);
      int ch = (l & 15) ^ (swz256(row) << 1);
      int col = j * 64 + (ch & 7) * 8;
      if (col >= DF) col -= 32;
      glds16((ch < 8 ? P.wg : P.wu) + (size_t)row * DF + col, (u32)(w * 32 + i) * 1024u);
    }
    wait_vm0();
    bar();
#pragma unroll
    for (int kk = 0; kk < 16; ++kk) {
#pragma unroll
      for (int fs = 0; fs < 2; ++fs) {
        Wg[fs][kk] = dense_frag(lds, kk * 32, 32 * (w >> 1) + 16 * fs + 4 * (l & 3), l);
        Wu[fs][kk] = dense_frag(lds, kk * 32, 64 + 32 * (w >> 1) + 16 * fs + 4 * (l & 3), l);
      }
    }
    bar();
  }
  const char* H2 = P.ws + OFF_H2;
  rsrc_t ra = mkrsrc(P.ws + OFF_ACT, NSEG * 2 * NFC * 1024);
  const int j32 = 2 * j + (w >> 1), tt = w & 1;
  Pred pq;
  pred_init(pq);
  for (int s = set; s < NSEG; s += 2) {
    if (w == 0) wait_pred(fl + F_H2 + s * (22 * 128) + j * 128, 32, l, pq);
    bar();
    if ((s >> 1) == SEGT) tl(P, 72);
    stage_rows<false>(H2 + (size_t)s * 32768, 0, w, l);
    wait_vm0();
    bar();
    if ((s >> 1) == SEGT) tl(P, 73);
    if (j32 < NFC) {
      f4 ga[2] = {{0, 0, 0, 0}, {0, 0, 0, 0}}, ua[2] = {{0, 0, 0, 0}, {0, 0, 0, 0}};
#pragma unroll
      for (int kk = 0; kk < 16; ++kk) {
        u4 h = lds_r128(lds + (tt * 16 + (l & 15)) * HROW + (l >> 4) * 16 + kk * 64);
#pragma unroll
        for (int fs = 0; fs < 2; ++fs) { ga[fs] = mfma32(Wg[fs][kk], h, ga[fs]); ua[fs] = mfma32(Wu[fs][kk], h, ua[fs]); }
      }
      float a[2][4];
#pragma unroll
      for (int fs = 0; fs < 2; ++fs)
#pragma unroll
        for (int c = 0; c < 4; ++c) a[fs][c] = bfr(gelu_tanh(bfr(ga[fs][c]))) * bfr(ua[fs][c]);
      bstore128(ra, (u32)((s * 2 + tt) * NFC + j32) * 1024u + l * 16,
                (u4){cvtpk(a[0][0], a[0][1]), cvtpk(a[0][2], a[0][3]), cvtpk(a[1][0], a[1][1]), cvtpk(a[1][2], a[1][3])});
    }
    if ((s >> 1) == SEGT) tl(P, 74);
    wait_vm0();
    bar();
    if (t < 16) st_flag(fl + F_ACT + s * 2048 + t * 128 + j * 4, 1u);
    tl(P, 8 + s);
  }
}

DEV void role_down(const Params& P, int tt, int dq, int set, ldsp lds) {
  const int t = threadIdx.x, l = t & 63, w = rfl(t >> 6);
  const int rho = l & 15, g = l >> 4;
  const char* fl = flag_base(P);
  u4 Wd[4][11];
#pragma unroll
  for (int c2 = 0; c2 < 2; ++c2) {
    for (int i = w; i < 86; i += 4) {
      int row = i * 16 + (l >> 2);
      int ch = (l & 3) ^ (((row >> 3) & 1) << 1);
      glds16(P.wd + (size_t)row * DM + dq * 64 + c2 * 32 + ch * 8, (u32)i * 1024u);
    }
    wait_vm0();
    bar();
    const int q = (l >> 2) & 3, p = l & 3;
#pragma unroll
    for (int i = 0; i < 11; ++i) {
      int j = min(w * 11 + i, NFC - 1);
#pragma unroll
      for (int tl = 0; tl < 2; ++tl) {
        u2 r[2];
#pragma unroll
        for (int hf = 0; hf < 2; ++hf) {
          int row = j * 32 + 16 * hf + 4 * g + q;
          int col = 8 * p + 4 * tl;
          int slot = (col >> 4) ^ ((row >> 3) & 1);
          r[hf] = trread(lds + row * 64 + slot * 32 + (col & 15) * 2);
        }
        u4 f = {r[0].x, r[0].y, r[1].x, r[1].y};
        if (w * 11 + i >= NFC) f = (u4){0, 0, 0, 0};
        Wd[c2 * 2 + tl][i] = f;
      }
    }
    bar();
  }
  rsrc_t ra = mkrsrc(P.ws + OFF_ACT, NSEG * 2 * NFC * 1024);
  rsrc_t rx1 = mkrsrc(P.ws + OFF_X1, S_ * DM * 2);
  rsrc_t rm = mkrsrc(P.ws + OFF_MOD, 3072 * 4);
  const int col0 = dq * 64 + (w & 1) * 32 + 8 * g;
  wait_words(fl + F_MOD + 80 * 4, 16, l);
  u4 gb0 = bload128(rm, 10240 + col0 * 4), gb1 = bload128(rm, 10240 + col0 * 4 + 16);
  Pred pq;
  pred_init(pq);
  for (int s = set; s < NSEG; s += 2) {
    if (w == 0) wait_pred(fl + F_ACT + s * 2048 + (dq * 2 + tt) * 128, 22, l, pq);
    bar();
    if ((s >> 1) == SEGT) tl(P, 72);
    u4 af[11];
#pragma unroll
    for (int i = 0; i < 11; ++i) {
      int j = min(w * 11 + i, NFC - 1);
      af[i] = bload128(ra, (u32)((s * 2 + tt) * NFC + j) * 1024u + l * 16);
    }
    const int row = s * 32 + tt * 16 + rho;
    u4 x1 = bload128(rx1, (row * DM + col0) * 2);
    __builtin_amdgcn_sched_barrier(0);
    f4 a[4] = {{0, 0, 0, 0}, {0, 0, 0, 0}, {0, 0, 0, 0}, {0, 0, 0, 0}};
#pragma unroll
    for (int i = 0; i < 11; ++i)
#pragma unroll
      for (int c = 0; c < 4; ++c) a[c] = mfma32(Wd[c][i], af[i], a[c]);
#pragma unroll
    for (int c = 0; c < 4; ++c) lds_wf4(lds + (w * 4 + c) * 1024 + l * 16, a[c]);
    if ((s >> 1) == SEGT) tl(P, 73);
    bar();
    if (w < 2) {
      f4 x = {0, 0, 0, 0}, y = {0, 0, 0, 0};
#pragma unroll
      for (int ww = 0; ww < 4; ++ww) {
        x += lds_rf4(lds + (ww * 4 + 2 * w) * 1024 + l * 16);
        y += lds_rf4(lds + (ww * 4 + 2 * w + 1) * 1024 + l * 16);
      }
      float o[8];
#pragma unroll
      for (int jx = 0; jx < 8; ++jx) {
        float f = jx < 4 ? x[jx & 3] : y[jx & 3];
        float gb = asf(jx < 4 ? gb0[jx & 3] : gb1[jx & 3]);
        float xx = (jx & 1) ? bfhi(x1[jx >> 1]) : bflo(x1[jx >> 1]);
        o[jx] = xx + bfr(gb * bfr(f));
      }
      bstore128(mkrsrc(P.out, S_ * DM * 2), (row * DM + col0) * 2, (u4){cvtpk(o[0], o[1]), cvtpk(o[2], o[3]), cvtpk(o[4], o[5]), cvtpk(o[6], o[7])});
      tl(P, 8 + s);
    }
  }
}

extern __shared__ __attribute__((aligned(16))) char smem[];
#define HALF 0
#define MAP 2
__global__ void __launch_bounds__(256, 1) k_fused(Params P) {
  const int x = (blockIdx.x & 7) ^ (HALF * 4), j = blockIdx.x >> 3;
  ldsp lds = (ldsp)smem;
  tl(P, 0);
  if (x < 4 && j < 8) { tl(P, 1, true); role_chain(P, j * 4 + x, chain_base(P)); tl(P, 2); tl(P, 3, true); return; }
  const int nearidx = (j - 8) * 4 + x, faridx = j * 4 + (x - 4);
  int id;
  id = x < 4 ? (nearidx < 32 ? 96 + nearidx : 128 + nearidx) : (faridx < 32 ? 128 + faridx : faridx - 32);
  if (id < 96) {
    const int idx = id;
    tl(P, 1);
    if (idx == 92) role_calib(P, smem, true, 1);
    role_mod(P, idx, smem);
    tl(P, 3);
    if (idx < 44) role_gu(P, idx % 22, idx / 22, lds);
    else if (idx < 76) role_down(P, (idx - 44) & 1, ((idx - 44) >> 1) & 7, (idx - 44) >> 4, lds);
    else if (idx < 92) role_red_early(P, idx - 76);
  } else {
    const int idx = id - 96;
    const int h = idx & 31, set = (idx >> 5) & 1;
    if (idx < 64) {
      role_kv(P, h, set, 2, lds, smem);
      if (set == 1) role_red_late(P, h, set, lds);
    } else {
      role_ln1(P, idx - 64, smem);
      tl(P, 1);
      role_att(P, h, set, 2, lds);
    }
  }
}

extern "C" void kernel_launch(void* const* d_in, const int* in_sizes, int n_in, void* d_out, int out_size, void* d_ws,
                              size_t ws_size, hipStream_t stream) {
  Params P;
  P.x = (const __hip_bfloat16*)d_in[0];
  P.mem0 = (const __hip_bfloat16*)d_in[1];
  P.Wq = (const __hip_bfloat16*)d_in[2];
  P.Wk = (const __hip_bfloat16*)d_in[3];
  P.Wv = (const __hip_bfloat16*)d_in[4];
  P.Wo = (const __hip_bfloat16*)d_in[5];
  P.wg = (const __hip_bfloat16*)d_in[8];
  P.wu = (const __hip_bfloat16*)d_in[9];
  P.wd = (const __hip_bfloat16*)d_in[10];
  P.c = (const __hip_bfloat16*)d_in[11];
  P.wmod = (const __hip_bfloat16*)d_in[12];
  P.out = (__hip_bfloat16*)d_out;
  P.ws = (char*)d_ws;
  const int LDSB = 163840;
  (void)hipFuncSetAttribute((const void*)k_fused, hipFuncAttributeMaxDynamicSharedMemorySize, LDSB);
  char* ch = (char*)((((size_t)((char*)d_ws + OFF_CH)) + 0x1fffff) & ~(size_t)0x1fffff);
  (void)hipMemsetAsync(ch, 0xff, FL_BASE + FL_SIZE, stream);
  k_fused<<<256, 256, LDSB, stream>>>(P);
}
```
